```python
import torch

def _softmax(x, axis=-1):
    return torch.softmax(x, dim=axis)
def _gelu(x):
    return torch.nn.functional.gelu(x, approximate="tanh")
def _silu(x):
    return torch.nn.functional.silu(x)
def _sigmoid(x):
    return torch.sigmoid(x)
def _layernorm(x, axis=-1, eps=1e-5):
    m = x.mean(dim=axis, keepdim=True)
    v = x.var(dim=axis, keepdim=True, unbiased=False)
    return (x - m) / torch.sqrt(v + eps)
def _scatter_add(tgt, idx, src, axis=0):
    return tgt.clone().index_add_(axis, idx, src)
def _segment_sum(src, seg, n):
    out = torch.zeros((n,) + src.shape[1:], dtype=src.dtype)
    return out.index_add_(0, seg, src)
def _pad(x, pw):
    flat = tuple(v for pair in reversed(pw) for v in pair)
    return torch.nn.functional.pad(x, flat)

kernel_name = 'sequential_preLN__linear_kernel_hedgehog__geglu_ffn__prompt'

def setup_inputs():

    _w = (lambda a: (lambda t: t.to(torch.bfloat16) if t.is_floating_point() else t)(torch.as_tensor(a)))
    def _A_setup():
        B, S, D, H = 1, 2048, 640, 32
        Dh = D // H
        return {
            "x": _w((lambda *s: torch.randn(*s, dtype=torch.bfloat16) * 0.05)(B, S, D)),
            "wqkv": _w((lambda *s: torch.randn(*s, dtype=torch.bfloat16) * 0.2)(D, 3 * D)),
            "w_phi": _w((lambda *s: torch.randn(*s, dtype=torch.bfloat16) * 0.2)(H, Dh, Dh) * (Dh ** -0.5)),
            "wo": _w((lambda *s: torch.randn(*s, dtype=torch.bfloat16) * 0.05)(D, D)),
        }
    _A_in = _A_setup()

    def _B_setup():
        B, S, D, F = 1, 2048, 640, 1728
        return {
            "x": _w((lambda *s: torch.randn(*s, dtype=torch.bfloat16) * 0.05)(B, S, D)),
            "w_gate": _w((lambda *s: torch.randn(*s, dtype=torch.bfloat16) * 0.05)(D, F)),
            "w_up": _w((lambda *s: torch.randn(*s, dtype=torch.bfloat16) * 0.05)(D, F)),
            "w_down": _w((lambda *s: torch.randn(*s, dtype=torch.bfloat16) * 0.05)(F, D)),
        }
    _B_in = _B_setup()

    return {
        "A_x": _A_in["x"],
        "A_wqkv": _A_in["wqkv"],
        "A_w_phi": _A_in["w_phi"],
        "A_wo": _A_in["wo"],
        "B_x": _B_in["x"],
        "B_w_gate": _B_in["w_gate"],
        "B_w_up": _B_in["w_up"],
        "B_w_down": _B_in["w_down"],
    }

def reference(A_x, A_wqkv, A_w_phi, A_wo, B_x, B_w_gate, B_w_up, B_w_down):
    def _A_ref(x, wqkv, w_phi, wo):
        B, S, D = x.shape
        H = 32; Dh = D // H
        qkv = (lambda x, *d: x.permute(*d))((x @ wqkv).reshape(B, S, 3, H, Dh), 2, 0, 3, 1, 4)
        q, k, v = qkv[0], qkv[1], qkv[2]
        pq = torch.einsum("bhsd,hdf->bhsf", q, w_phi)
        pk = torch.einsum("bhsd,hdf->bhsf", k, w_phi)
        phi_q = torch.concatenate([_softmax(pq, axis=-1), _softmax(-pq, axis=-1)], axis=-1)
        phi_k = torch.concatenate([_softmax(pk, axis=-1), _softmax(-pk, axis=-1)], axis=-1)
        kv = torch.einsum("bhsf,bhsd->bhfd", phi_k, v)
        z  = torch.einsum("bhsf->bhf", phi_k)
        num = torch.einsum("bhqf,bhfd->bhqd", phi_q, kv)
        den = torch.einsum("bhqf,bhf->bhq", phi_q, z)[..., None] + 1e-6
        o = num / den
        return (lambda x, *d: x.permute(*d))(o, 0, 2, 1, 3).reshape(B, S, D) @ wo

    def _B_ref(x, w_gate, w_up, w_down):
        return (_gelu(x @ w_gate) * (x @ w_up)) @ w_down

    x = A_x
    h = x + _A_ref(_layernorm(x, axis=-1), A_wqkv, A_w_phi, A_wo)
    h2 = h + _B_ref(_layernorm(h, axis=-1), B_w_gate, B_w_up, B_w_down)
    return h2
```

```cpp
#include <hip/hip_runtime.h>
#include <hip/hip_bf16.h>
#include <stdint.h>

typedef unsigned short u16;
static __device__ __forceinline__ float bf2f(u16 b){ return __uint_as_float(((unsigned)b)<<16); }
static __device__ __forceinline__ u16 f2bf(float f){ __hip_bfloat16 h = __float2bfloat16(f); return *(u16*)&h; }
static __device__ __forceinline__ float rbf(float f){ return bf2f(f2bf(f)); }

constexpr int S=2048, D=640, H=32, DH=20, F=1728;

__global__ void ln_k(const u16* x, u16* y){
  int row = blockIdx.x; int t = threadIdx.x;
  float v[10]; float s=0;
  for(int i=0;i<10;i++){ v[i]=bf2f(x[row*D+t+64*i]); s+=v[i]; }
  for(int o=32;o>0;o>>=1) s+=__shfl_xor(s,o);
  float m=s/D; float q=0;
  for(int i=0;i<10;i++){ q+=(v[i]-m)*(v[i]-m);}
  for(int o=32;o>0;o>>=1) q+=__shfl_xor(q,o);
  float r=rsqrtf(q/D+1e-5f);
  for(int i=0;i<10;i++) y[row*D+t+64*i]=f2bf((v[i]-m)*r);
}
__global__ void gemm_k(const u16* A, const u16* B, u16* C, const u16* res, int M, int N, int K){
  int n = blockIdx.x*64 + (threadIdx.x&63); int m = blockIdx.y*4 + (threadIdx.x>>6);
  if(n>=N) return;
  float acc=0;
  for(int k=0;k<K;k++) acc += bf2f(A[m*K+k])*bf2f(B[k*N+n]);
  acc = rbf(acc);
  if(res) acc += bf2f(res[m*N+n]);
  C[m*N+n]=f2bf(acc);
}
__global__ void phi_k(const u16* qkv, const u16* wphi, u16* phiq, u16* phik){
  int idx = blockIdx.x*blockDim.x+threadIdx.x;
  int s = idx/64, w=(idx/32)&1, h=idx&31;
  float p[DH];
  for(int f=0;f<DH;f++){ float a=0; for(int d=0;d<DH;d++) a+=bf2f(qkv[s*1920+w*640+h*DH+d])*bf2f(wphi[(h*DH+d)*DH+f]); p[f]=rbf(a);}
  float mx=-1e30f, mn=1e30f; for(int f=0;f<DH;f++){mx=fmaxf(mx,p[f]); mn=fminf(mn,p[f]);}
  float s1=0,s2=0; for(int f=0;f<DH;f++){ s1+=__expf(p[f]-mx); s2+=__expf(mn-p[f]); }
  u16* o = (w? phik: phiq) + (s*H+h)*40;
  for(int f=0;f<DH;f++){ o[f]=f2bf(__expf(p[f]-mx)/s1); o[20+f]=f2bf(__expf(mn-p[f])/s2);}
}
__global__ void kv_k(const u16* qkv, const u16* phik, float* kv){
  int idx = blockIdx.x*blockDim.x+threadIdx.x;
  if(idx>=H*40*21) return;
  int h=idx/840, f=(idx%840)/21, d=idx%21;
  float a=0;
  for(int s=0;s<S;s++){ float p=bf2f(phik[(s*H+h)*40+f]); float v = d<20? bf2f(qkv[s*1920+1280+h*DH+d]) : 1.f; a+=p*v; }
  kv[idx]=rbf(a);
}
__global__ void o_k(const u16* phiq, const float* kv, u16* o){
  int idx = blockIdx.x*blockDim.x+threadIdx.x;
  int s=idx/640, h=(idx%640)/20, d=idx%20;
  float num=0, den=0;
  for(int f=0;f<40;f++){ float p=bf2f(phiq[(s*H+h)*40+f]); num+=p*kv[h*840+f*21+d]; den+=p*kv[h*840+f*21+20]; }
  num=rbf(num); den=rbf(rbf(den)+1e-6f);
  o[idx]=f2bf(num/den);
}
__global__ void geglu_k(const u16* g, const u16* u, u16* a){
  int idx = blockIdx.x*blockDim.x+threadIdx.x;
  float x=bf2f(g[idx]);
  float t=tanhf(0.7978845608f*(x+0.044715f*x*x*x));
  float ge=rbf(0.5f*x*(1.f+t));
  a[idx]=f2bf(ge*bf2f(u[idx]));
}

extern "C" void kernel_launch(void* const* d_in, const int* in_sizes, int n_in,
                              void* d_out, int out_size, void* d_ws, size_t ws_size,
                              hipStream_t stream) {
  const u16* x=(const u16*)d_in[0]; const u16* wqkv=(const u16*)d_in[1]; const u16* wphi=(const u16*)d_in[2]; const u16* wo=(const u16*)d_in[3];
  const u16* wg=(const u16*)d_in[5]; const u16* wu=(const u16*)d_in[6]; const u16* wd=(const u16*)d_in[7];
  u16* out=(u16*)d_out;
  char* ws=(char*)d_ws; size_t MB=1<<20;
  u16* xn=(u16*)(ws); u16* qkv=(u16*)(ws+8*MB); u16* phiq=(u16*)(ws+24*MB); u16* phik=(u16*)(ws+32*MB);
  float* kv=(float*)(ws+40*MB); u16* o=(u16*)(ws+48*MB); u16* h=(u16*)(ws+56*MB); u16* g=(u16*)(ws+64*MB); u16* u=(u16*)(ws+80*MB); u16* a=(u16*)(ws+96*MB);
  ln_k<<<S,64,0,stream>>>(x,xn);
  gemm_k<<<dim3(1920/64,S/4),256,0,stream>>>(xn,wqkv,qkv,nullptr,S,1920,D);
  phi_k<<<S*64/256,256,0,stream>>>(qkv,wphi,phiq,phik);
  kv_k<<<(H*840+255)/256,256,0,stream>>>(qkv,phik,kv);
  o_k<<<S*640/256,256,0,stream>>>(phiq,kv,o);
  gemm_k<<<dim3(640/64,S/4),256,0,stream>>>(o,wo,h,x,S,640,D);
  ln_k<<<S,64,0,stream>>>(h,xn);
  gemm_k<<<dim3(F/64,S/4),256,0,stream>>>(xn,wg,g,nullptr,S,F,D);
  gemm_k<<<dim3(F/64,S/4),256,0,stream>>>(xn,wu,u,nullptr,S,F,D);
  geglu_k<<<S*F/256,256,0,stream>>>(g,u,a);
  gemm_k<<<dim3(640/64,S/4),256,0,stream>>>(a,wd,out,h,S,640,F);
}
```

```cpp
typedef short bf16x8 __attribute__((ext_vector_type(8)));
DI f32x16 bm32(v4u a, v4u b, f32x16 c){ return __builtin_amdgcn_mfma_f32_32x32x16_bf16(__builtin_bit_cast(bf16x8,a),__builtin_bit_cast(bf16x8,b),c,0,0,0); }
DI f32x4 bm16(v4u a, v4u b, f32x4 c){ return __builtin_amdgcn_mfma_f32_16x16x32_bf16(__builtin_bit_cast(bf16x8,a),__builtin_bit_cast(bf16x8,b),c,0,0,0); }
DI v2u trd(unsigned addr){ return __builtin_bit_cast(v2u, __builtin_amdgcn_ds_read_tr16_b64_v4i16((LAS s4*)addr)); }
DI v4u trfrag(unsigned a0, unsigned a1){ v2u x=trd(a0), y=trd(a1); return v4u{x.x,x.y,y.x,y.y}; }
DI v4u mkrs(const void* p, unsigned bytes){ u64 q=(u64)p; return v4u{(unsigned)q,(unsigned)(q>>32)&0xffffu,bytes,0x00020000u}; }
DI void dma0(v4u rs, unsigned lds, unsigned voff, unsigned soff){
  asm volatile("s_mov_b32 m0, %0\n\ts_nop 0\n\tbuffer_load_dwordx4 %1, %2, %3 offen lds" :: "s"(lds), "v"(voff), "s"(rs), "s"(soff) : "memory");
}
DI void st8(v2u d, v4u rs, unsigned voff, unsigned soff){
  asm volatile("buffer_store_dwordx2 %0, %1, %2, %3 offen sc1" :: "v"(d), "v"(voff), "s"(rs), "s"(soff) : "memory");
}
DI void st4(unsigned d, v4u rs, unsigned voff, unsigned soff){
  asm volatile("buffer_store_dword %0, %1, %2, %3 offen sc1" :: "v"(d), "v"(voff), "s"(rs), "s"(soff) : "memory");
}
DI void ld8(v2u& d, v4u rs, unsigned voff, unsigned soff){
  asm volatile("buffer_load_dwordx2 %0, %1, %2, %3 offen sc1" : "=v"(d) : "v"(voff), "s"(rs), "s"(soff) : "memory");
}
DI void lgk0(){ asm volatile("s_waitcnt lgkmcnt(0)" ::: "memory"); }
DI float dot2(unsigned a, unsigned b, float c){ return __builtin_amdgcn_fdot2_f32_bf16(__builtin_bit_cast(bf2,a),__builtin_bit_cast(bf2,b),c,false); }
DI float xh_max(float v){ auto r=__builtin_amdgcn_permlane32_swap(__float_as_uint(v),__float_as_uint(v),false,false); return fmaxf(__uint_as_float(r[0]),__uint_as_float(r[1])); }
DI float xh_min(float v){ auto r=__builtin_amdgcn_permlane32_swap(__float_as_uint(v),__float_as_uint(v),false,false); return fminf(__uint_as_float(r[0]),__uint_as_float(r[1])); }
DI float xh_add(float v){ auto r=__builtin_amdgcn_permlane32_swap(__float_as_uint(v),__float_as_uint(v),false,false); return __uint_as_float(r[0])+__uint_as_float(r[1]); }
DI float x16_add(float v){ auto r=__builtin_amdgcn_permlane16_swap(__float_as_uint(v),__float_as_uint(v),false,false); return __uint_as_float(r[0])+__uint_as_float(r[1]); }
constexpr unsigned OOB = 0x80000000u;
constexpr unsigned L_KV=0, L_XS=51200, XS_W=28160, L_WQ=102400, L_EX=153600;
constexpr unsigned L_PHI=51200, L_VI=L_PHI+128*160, L_CSX=L_EX, L_KVI=L_EX+2048, L_WO=51200, L_RT=0;
constexpr unsigned KVP_B=3584;
constexpr float LOG2E=1.4426950408889634f;

DI void hedgehog(const f32x16& p, unsigned (&o)[10]){
  float mx=p[0], mn=p[0];
  #pragma unroll
  for(int i=1;i<10;i++){ mx=fmaxf(mx,p[i]); mn=fminf(mn,p[i]); }
  mx=xh_max(mx)*LOG2E; mn=xh_min(mn)*LOG2E;
  float ep[10], en[10], sp=0.f, sn=0.f;
  #pragma unroll
  for(int i=0;i<10;i++){ ep[i]=__builtin_amdgcn_exp2f(__builtin_fmaf(p[i],LOG2E,-mx)); en[i]=__builtin_amdgcn_exp2f(__builtin_fmaf(p[i],-LOG2E,mn)); sp+=ep[i]; sn+=en[i]; }
  sp=__builtin_amdgcn_rcpf(xh_add(sp)); sn=__builtin_amdgcn_rcpf(xh_add(sn));
  #pragma unroll
  for(int i=0;i<5;i++){ o[i]=pk2(ep[2*i]*sp,ep[2*i+1]*sp); o[5+i]=pk2(en[2*i]*sn,en[2*i+1]*sn); }
}
DI v4u pack8(const f32x16& a, int r0, float s){ return v4u{pk2(a[r0]*s,a[r0+1]*s),pk2(a[r0+2]*s,a[r0+3]*s),pk2(a[r0+4]*s,a[r0+5]*s),pk2(a[r0+6]*s,a[r0+7]*s)}; }
DI v4u cs_frag(unsigned lb, int t, int lane){
  f32x4 v=*(const LAS f32x4*)(lb+L_CSX+t*512+(lane&31)*16);
  float s=(v[0]+v[1])+(v[2]+v[3]);
  unsigned hi=pk2(s,s); float lo=s-blo(hi); unsigned l2=pk2(lo,0.f);
  return lane<32 ? v4u{hi,l2,0u,0u} : v4u{0u,0u,0u,0u};
}
#define MAPM 4
#define CODE_BYTES 81920
#define ROT 1
template<int I> using IC = std::integral_constant<int,I>;
template<int N,int DP,class RD,class MM,class SD> DI void pipe(RD rd, MM mm, SD sd){
  v4u A[DP+1];
  [&]<int... I>(std::integer_sequence<int,I...>){ ((A[I]=rd(IC<I>{})), ...); }(std::make_integer_sequence<int,DP>{});
  SB0();
  auto step=[&](auto i){ constexpr int I=i; mm(i,A[I%(DP+1)]); SB0(); sd(i); if constexpr(I+DP<N) A[(I+DP)%(DP+1)]=rd(IC<I+DP>{}); SB0(); };
  [&]<int... I>(std::integer_sequence<int,I...>){ (step(IC<I>{}), ...); }(std::make_integer_sequence<int,N>{});
}
DI void mfma32vv_0(f32x16& c, v4u a, v4u x){ asm volatile("v_mfma_f32_32x32x16_bf16 %0, %1, %2, 0" : "=&v"(c) : "v"(a), "v"(x)); }
DI void mfma32vv(f32x16& c, v4u a, v4u x){ asm volatile("v_mfma_f32_32x32x16_bf16 %0, %1, %2, %0" : "+v"(c) : "v"(a), "v"(x)); }
DI void mfma32av_0(f32x16& c, v4u a, v4u x){ asm volatile("v_mfma_f32_32x32x16_bf16 %0, %1, %2, 0" : "=&v"(c) : "a"(a), "v"(x)); }
DI void mfma16a_0(f32x4& c, v4u a, v4u x){ asm volatile("v_mfma_f32_16x16x32_bf16 %0, %1, %2, 0" : "=&v"(c) : "a"(a), "v"(x)); }
DI void nop2(){ asm volatile("s_nop 1" ::: "memory"); }
DI void mdone(f32x16& c){ asm volatile("s_nop 11" : "+v"(c)); }
template<int OFF> DI void ldsrdA(v4u& d, unsigned a){ asm volatile("ds_read_b128 %0, %1 offset:%2" : "=a"(d) : "v"(a), "n"(OFF) : "memory"); }
typedef unsigned v2ua __attribute__((ext_vector_type(2)));
template<int OFF,int ST> DI void trA(v4u& d, unsigned a){
  v2u x,y;
  asm volatile("ds_read_b64_tr_b16 %0, %2 offset:%3\n\tds_read_b64_tr_b16 %1, %2 offset:%4" : "=a"(x), "=a"(y) : "v"(a), "n"(OFF), "n"(OFF+ST) : "memory");
  d=v4u{x.x,x.y,y.x,y.y};
}
DI void hedgehog2(f32x16 p, f32x16 r, bool hh, float sc, unsigned (&o)[2][10]){
  p[8]=hh? p[10]:p[8]; p[9]=hh? p[11]:p[9]; r[8]=hh? r[10]:r[8]; r[9]=hh? r[11]:r[9];
  float mx[2], mn[2];
  #pragma unroll
  for(int a=0;a<2;a++){ const f32x16& z=a? r:p;
    mx[a]=fmaxf(fmaxf(fmaxf(fmaxf(z[0],z[1]),z[2]),fmaxf(fmaxf(z[3],z[4]),z[5])),fmaxf(fmaxf(fmaxf(z[6],z[7]),z[8]),z[9]));
    mn[a]=fminf(fminf(fminf(fminf(z[0],z[1]),z[2]),fminf(fminf(z[3],z[4]),z[5])),fminf(fminf(fminf(z[6],z[7]),z[8]),z[9])); }
  #pragma unroll
  for(int a=0;a<2;a++){ mx[a]=-xh_max(mx[a])*sc; mn[a]=xh_min(mn[a])*sc; }
  f32x2 ep[2][5], en[2][5];
  #pragma unroll
  for(int a=0;a<2;a++){ const f32x16& z=a? r:p;
    #pragma unroll
    for(int i=0;i<5;i++){ f32x2 v={z[2*i],z[2*i+1]};
      f32x2 u=__builtin_elementwise_fma(v,f32x2{sc,sc},f32x2{mx[a],mx[a]}), t=__builtin_elementwise_fma(v,f32x2{-sc,-sc},f32x2{mn[a],mn[a]});
      ep[a][i]=f32x2{__builtin_amdgcn_exp2f(u[0]),__builtin_amdgcn_exp2f(u[1])}; en[a][i]=f32x2{__builtin_amdgcn_exp2f(t[0]),__builtin_amdgcn_exp2f(t[1])}; } }
  #pragma unroll
  for(int a=0;a<2;a++){
    f32x2 sp=(ep[a][0]+ep[a][1])+(ep[a][2]+ep[a][3])+ep[a][4], sn=(en[a][0]+en[a][1])+(en[a][2]+en[a][3])+en[a][4];
    float rp=__builtin_amdgcn_rcpf(xh_add(sp[0]+sp[1])), rn=__builtin_amdgcn_rcpf(xh_add(sn[0]+sn[1]));
    #pragma unroll
    for(int i=0;i<5;i++){ f32x2 u=ep[a][i]*rp, t=en[a][i]*rn; o[a][i]=pk2(u[0],u[1]); o[a][5+i]=pk2(t[0],t[1]); } }
}
DI v4u pack8r(const f32x16& a, int r0){ return v4u{pk2(a[r0],a[r0+1]),pk2(a[r0+2],a[r0+3]),pk2(a[r0+4],a[r0+5]),pk2(a[r0+6],a[r0+7])}; }

struct Args { const u16 *x,*wqkv,*wphi,*wo,*wg,*wu,*wd; u16* out; char* ws; };
constexpr unsigned M_KV=0, M_XS=102400, M_WQ=0, M_PHI=51200, M_VI=M_PHI+128*160, M_WO=51200, M_RT=122880;
constexpr unsigned M_CSX=L_CSX, M_KVI=L_KVI, M_WP=L_KVI+4224;
template<int OFF> DI void dmaN(v4u rs, unsigned voff, unsigned soff){
  asm volatile("buffer_load_dwordx4 %0, %1, %2 offen offset:%3 lds" :: "v"(voff), "s"(rs), "s"(soff), "n"(OFF) : "memory");
}
DI void touch(unsigned& d, v4u rs, unsigned voff, unsigned soff){
  asm volatile("buffer_load_dword %0, %1, %2, %3 offen" : "=v"(d) : "v"(voff), "s"(rs), "s"(soff) : "memory");
}

__global__ __launch_bounds__(256,1) void blk_k(Args ar){
  extern __shared__ __attribute__((aligned(16))) char smem[];
  char* ws=ar.ws;
  Ctx c;
  c.ws=ws; c.b=blockIdx.x; c.lane=threadIdx.x&63; c.w=__builtin_amdgcn_readfirstlane(threadIdx.x>>6);
  c.pi=((c.b&7)>>1)*4+((c.b>>3)&3); c.pj=(c.b&1)*8+(c.b>>5);
  const int lane=c.lane, w=c.w, pi=c.pi, pj=c.pj, jq=pj^1, tid=threadIdx.x;
  const unsigned lb=0;
  stamp(ws,c.b,0);
  const v4u rx=mkrs(ar.x,S*D*2), rw=mkrs(ar.wqkv,D*1920*2);
  const int rot = ROT? (3*pi+pj)%10 : 0;
  auto kphys=[&](int kc){ int k=kc+rot; return k>=10? k-10:k; };
  const unsigned xs=lb+M_XS+w*12800;
  unsigned xv[4]; for(int e=0;e<4;e++) xv[e]=(lane>>3)*1280+(((lane&7)^((lane>>4)+4*(e&1))))*16+e*(10240-1024);
  const unsigned xrow=(pi*128+w*32)*1280;
  auto xpiece=[&](int kc,auto RQ){ constexpr int rq=RQ; unsigned so=xrow+kphys(kc)*128;
    if constexpr(rq==0) dma0(rx, xs+(kc%3)*4096, xv[0], so); else dmaN<rq*1024>(rx,xv[rq],so); };
  UNROLL(4,I,xpiece(0,IC<I>{}));
  unsigned wv[3];
  for(int p=0;p<3;p++){ int idx=p*64+lane, n=(idx*205)>>11, ch=idx-n*10; int row=(n&8)+4*(n&1)+((n>>1)&3); wv[p]=row*3840+(ch<5? 1280+ch*16 : 2480+ch*16)-p*1024; }
  auto kvpiece=[&](int kc,auto P){ constexpr int p=P; unsigned so=(kphys(kc)*4+w)*16*3840+pj*80;
    if constexpr(p==0) dma0(rw, lb+M_KV+(kc*4+w)*2560, wv[0], so); else if constexpr(p==1) dmaN<1024>(rw,wv[1],so); else { if(lane<32) dmaN<2048>(rw,wv[2],so); } };
  UNROLL(3,I,kvpiece(0,IC<I>{}));
  UNROLL(4,I,xpiece(1,IC<I>{})); UNROLL(3,I,kvpiece(1,IC<I>{}));
  c.rs=mkrs(ws,WS_END); c.voff=lane*16; c.nretry=0;
  const int tok=lane&31, hh=lane>>5, tk=lane&15, g4=lane>>4;
  const int cc=pj&7, rh=pj>>3, th=w>>1, kh=w&1, lid=pi*16+pj;
  v2u ep; ld8(ep,c.rs,0u,WS_EPOCH);
  if(lane<50) dma0(mkrs(ar.wphi,NH*400*2), lb+M_WP+w*800, c.voff, (pj>>1)*3200+w*800);
  unsigned dummy;
  { u64 pc; unsigned len; asm volatile("s_getpc_b64 %0\n.Lhg_code_begin:\n\ts_mov_b32 %1, .Lhg_code_end-.Lhg_code_begin" : "=s"(pc), "=s"(len));
    touch(dummy, mkrs((const void*)pc,len), lane<5? ((c.b>>3)*20+w*5+lane)*128:OOB, 0); }
  v4u PZ={POISON,POISON,POISON,POISON}; pinV(PZ);
  unsigned ov=WS_O+((pi*4+w)*2+(tok>>4))*20480+jq*1280+(tok&15)*16, ov1=ov+hh*256, ov2=hh? OOB:ov+512;
  st16(PZ,c.rs,tid<220? tid*16:OOB, WS_KVP+(pj*16+pi)*KVP_B);
  st16(PZ,c.rs,ov1,0); st16(PZ,c.rs,ov1,768); st16(PZ,c.rs,ov2,0);
  PIN(ov1); PIN(ov2);
  unsigned qv[5], ovv[5];
  for(int p=0;p<5;p++){ int idx=p*64+lane, n=(idx*205)>>10, ch=idx-n*5; int row=(n&~15)+4*(n&3)+((n>>2)&3); qv[p]=row*3840+ch*16-(p&3)*1024; PIN(qv[p]); }
  for(int p=0;p<5;p++){ int idx=p*64+lane, n=(idx*205)>>11, ch=idx-n*10; int row=(n&~12)|((n&4)<<1)|((n&8)>>1); ovv[p]=row*1280+ch*16-(p&3)*1024; PIN(ovv[p]); }
  auto wqpiece=[&](int r,auto P){ constexpr int p=P; int grp=r*4+w; if(grp>=10) grp-=4; unsigned so=kphys(grp)*64*3840+jq*80;
    if constexpr(p%4==0) dma0(rw, lb+M_WQ+grp*5120+p*1024, qv[p], so); else dmaN<p*1024>(rw,qv[p],so); };
  float sm[2]={0.f,0.f}, sq[2]={0.f,0.f};
  unsigned C11=0x3f803f80u, C01=0x00003f80u, C10=0x3f800000u; PIN(C11); PIN(C01); PIN(C10);
  unsigned xr[4]; for(int s=0;s<4;s++){ xr[s]=xs+tok*128+(((2*s+hh)^((tok>>1)&7))*16); PIN(xr[s]); }
  const int blk=(lane>>4)&1, q4=(lane&15)>>2, p4=lane&3, qd=blk*4+p4;
  v4u ONE = tok==0 ? v4u{0x3f803f80u,0x3f803f80u,0x3f803f80u,0x3f803f80u} : v4u{0,0,0,0}; pinV(ONE);
  unsigned ab[2][3], aw[2][3];
  { const unsigned rb=lb+M_KV+160*(8*hh+2*q4);
    ab[0][0]=rb+8*(qd<5? qd: qd+8); ab[0][1]=rb+8*(qd+5); ab[0][2]=rb+8*(16+(qd&3));
    for(int t=0;t<3;t++){ ab[1][t]=ab[0][t]+51200; aw[0][t]=ab[0][t]+w*2560; aw[1][t]=aw[0][t]+51200; PIN(ab[0][t]); PIN(ab[1][t]); PIN(aw[0][t]); PIN(aw[1][t]); } }
  if(tid<88) *(LAS v2u*)(lb+M_KVI+tid*48+40)=v2u{0,0};
  const bool job = lid<216;
  const int jl=lid%27, jp=lid/27;
  SB0();
  stamp(ws,c.b,1);
  f32x16 acc[3], acs[3]; v4u xt[4]; v4u PW_[4][2];
  auto stats=[&]{ for(int s=0;s<4;s++){ v4u v=xt[s]; sm[0]=dot2(v.x,C11,sm[0]); sq[0]=dot2(v.x,v.x,sq[0]); sm[1]=dot2(v.y,C11,sm[1]); sq[1]=dot2(v.y,v.y,sq[1]);
      sm[0]=dot2(v.z,C11,sm[0]); sq[0]=dot2(v.z,v.z,sq[0]); sm[1]=dot2(v.w,C11,sm[1]); sq[1]=dot2(v.w,v.w,sq[1]); } PIN(sm[0]); PIN(sm[1]); PIN(sq[0]); PIN(sq[1]); };
  auto chunk=[&](auto KC){ constexpr int kc=KC, hf=kc>=5, o0=(kc-5*hf)*10240;
    if constexpr(kc<2) vmwait<14>(); else if constexpr(kc<9) vmwait<7>(); else vmwait<5>();
    barrier();
    if constexpr(kc==2){ asm volatile("" : "+v"(ep),"+v"(dummy));
      c.tag = (((u64)__builtin_amdgcn_readfirstlane(ep.y)<<32)|__builtin_amdgcn_readfirstlane(ep.x)) + 0x9E3779B97F4A7C15ull;
      if(tid==0) __hip_atomic_store((GAS u64*)(ws+WS_FLAG+(FK_A*256+lid)*128), c.tag, RLX); }
    ldsrdA<(kc%3)*4096>(c.X[kc*4+0],xr[0]); ldsrdA<(kc%3)*4096>(c.X[kc*4+1],xr[1]); ldsrdA<(kc%3)*4096>(c.X[kc*4+2],xr[2]); ldsrdA<(kc%3)*4096>(c.X[kc*4+3],xr[3]);
    v4u A[5];
    auto rd=[&](auto i){ constexpr int r=i, t=r%3; unsigned a=(r<3? aw[hf][t]+o0 : ab[hf][t]+o0+((r-3)/3)*2560); return trfrag(a,a+160); };
    A[0]=rd(IC<0>{}); A[1]=rd(IC<1>{}); A[2]=rd(IC<2>{}); A[3]=rd(IC<3>{});
    SB0();
    if constexpr(kc>0) stats();
    SB0();
    auto step=[&](auto i){ constexpr int r=i, t=r%3;
      if constexpr(r<3){ if constexpr(kc==0) mfma32vv_0(acs[t],ONE,A[r%5]); else mfma32vv(acs[t],ONE,A[r%5]); }
      else { if constexpr(kc==0 && r<6) mfma32_0(acc[t],A[r%5],c.X[0]); else mfma32(acc[t],A[r%5],c.X[kc*4+(r-3)/3]); }
      SB0();
      if constexpr(r+4<15) A[(r+4)%5]=rd(IC<r+4>{});
      if constexpr(r>=11) xt[r-11]=*(const LAS v4u*)(xr[r-11]+(kc%3)*4096);
      if constexpr(kc<8 && r%2==0 && r<14){ if constexpr(r<8) xpiece(kc+2,IC<(r/2)&3>{}); else kvpiece(kc+2,IC<(r/2-4)%3>{}); }
      if constexpr(kc>=8 && r%2==0 && r<10) wqpiece(kc-8,IC<(r/2)%5>{});
      if constexpr(kc==3 && r<12){ constexpr int a=r/3, e=r%3;
        unsigned p=lb+M_WP+((a<2? (pj&1):(jq&1))*2+(a&1))*800+(e<2? (8*e+4*hh+q4):(16+q4))*40+8*(qd<4? qd:4);
        v2u v=trd(p);
        if constexpr(e==0){ PW_[a][0].x=v.x; PW_[a][0].y=v.y; } else if constexpr(e==1){ PW_[a][0].z=v.x; PW_[a][0].w=v.y; } else { PW_[a][1]=v4u{v.x,v.y,0u,0u}; } }
      if constexpr(kc==4 && r<4){ PW_[r][1].x= hh? 0u:PW_[r][1].x; PW_[r][1].y= hh? 0u:PW_[r][1].y; pinA(PW_[r][0]); pinA(PW_[r][1]); }
      SB0(); };
    UNROLL(15,I,step(IC<I>{}));
  };
  UNROLL(10,I,chunk(IC<I>{}));
  stamp(ws,c.b,2);
  if(lane<32){ asm volatile("" : "+v"(acs[0]),"+v"(acs[1]),"+v"(acs[2])); for(int t=0;t<3;t++) *(LAS float*)(lb+M_CSX+t*512+lane*16+w*4)=acs[t][0]; }
  UNROLL(5,I,wqpiece(2,IC<I>{}));
  stats();
  float rstd1; v4u MB1;
  { float s1=xh_add(sm[0]+sm[1]), s2=xh_add(sq[0]+sq[1]); float mean=s1*(1.f/640.f); float var=fmaxf(s2*(1.f/640.f)-mean*mean,0.f)+1e-5f; rstd1=__builtin_amdgcn_rsqf(var);
    unsigned hi=pk2(-mean,0.f); float lo=-mean-blo(hi); unsigned a=pk2(-mean,lo);
    MB1 = hh? v4u{0,0,0,0} : v4u{a,hi,0,0}; pinV(MB1); PIN(rstd1); }
  lgk0(); barrier();
  stamp(ws,c.b,23);
  const v4u ro=mkrs(ar.wo,D*D*2);
  { v4u cf[3]; for(int t=0;t<3;t++){ cf[t]=cs_frag(lb,t,lane); pinV(cf[t]); }
    nop2(); for(int t=0;t<3;t++) mfma32vv(acc[t],cf[t],MB1);
    { const v4u rg=mkrs(w&1? ar.wu:ar.wg,job? D*F*2:0), rd=mkrs(ar.wd,job? D*F*2:0); const unsigned vo=(lane>>3)*3456+(lane&7)*16;
      for(int r=0;r<5;r++){ int rr=(w>>1)*5+r; dma0(rg,lb+M_RT+(w&1)*10240+rr*1024,vo,(jp*80+rr*8)*3456+jl*128); }
      const int ju=lid*2+(w>>1);
      for(int p=0;p<3;p++){ int idx=p*64+lane, n=(idx*205)>>11, ch=idx-n*10;
        if(p<2 || lane<32) dma0(rd,lb+M_RT+20480+w*2560+p*1024,n*1280+ch*16,((ju>>3)*32+(w&1)*16)*1280+(ju&7)*160); }
      touch(dummy,ro,lane<50? ((c.b>>3)*200+w*50+lane)*128:OOB,0); }
    const unsigned prow=lb+M_PHI+(w*32+tok)*160+hh*40, vrow=lb+M_VI+(w*32+tok)*96;
    const float vs=hh? rstd1:1.f;
    f32x16 pz[2]; v4u k0[2],k1[2];
    SB0();
    #pragma unroll
    for(int a=0;a<2;a++){ asm volatile("" : "+v"(acc[a]));
      k0[a]=pack8r(acc[a],0);
      k1[a]=v4u{pk2(acc[a][8]*vs,acc[a][9]*vs),pk2(acc[a][10]*vs,acc[a][11]*vs),pk2(acc[a][12]*rstd1,acc[a][13]*rstd1),pk2(acc[a][14]*rstd1,acc[a][15]*rstd1)};
      pinV(k0[a]); pinV(k1[a]); }
    nop2();
    for(int a=0;a<2;a++){ mfma32av_0(pz[a],PW_[a][0],k0[a]); mfma32av(pz[a],PW_[a][1],k1[a]); }
    SB0();
    #pragma unroll
    for(int a=0;a<2;a++){
      *(LAS v2u*)(vrow+(hh? (a==0? 24:0) : a*48+40)) = hh? v2u{k1[a].x,k1[a].y} : v2u{0x00003f80u,0u};
      *(LAS v2u*)(vrow+(a==0? (hh? 48:32) : (hh? 16:8))) = v2u{k1[a].z,k1[a].w}; }
    asm volatile("" : "+v"(acc[2]));
    v4u v2=pack8(acc[2],0,rstd1);
    *(LAS v2u*)(vrow+56+hh*8)=v2u{v2.x,v2.y}; *(LAS v2u*)(vrow+72+hh*8)=v2u{v2.z,v2.w};
    SB0(); asm volatile("s_nop 3" : "+v"(pz[0]),"+v"(pz[1]));
    unsigned ph[2][10]; hedgehog2(pz[0],pz[1],hh,rstd1*LOG2E,ph);
    for(int a=0;a<2;a++) for(int i=0;i<5;i++) *(LAS v2u*)(prow+a*80+i*8)=v2u{ph[a][2*i],ph[a][2*i+1]};
  }
  vmwait<9>(); lgk0(); barrier();
  stamp(ws,c.b,3);
  { unsigned pa=lb+M_PHI+(8*hh+q4)*160+(w>>1)*80+(w&1)*40+8*(qd<5?qd:4), va=lb+M_VI+(8*hh+q4)*96+(w>>1)*48+8*(qd<6?qd:5);
    PIN(pa); PIN(va);
    v4u fa[8],fb[8];
    #pragma unroll
    for(int s=0;s<8;s++){ fa[s]=trfrag(pa+s*2560,pa+s*2560+640); fb[s]=trfrag(va+s*1536,va+s*1536+384); }
    SB0();
    f32x16 kv; mfma32vv_0(kv,fa[0],fb[0]);
    #pragma unroll
    for(int s=1;s<8;s++) mfma32vv(kv,fa[s],fb[s]);
    mdone(kv);
    const unsigned so=WS_KVP+(pj*16+pi)*KVP_B+w*880, vo=tok<22? tok*8+hh*176 : OOB;
    st8(v2u{pk2(kv[0],kv[1]),pk2(kv[2],kv[3])},c.rs,vo,so);
    st8(v2u{pk2(kv[4],kv[5]),pk2(kv[6],kv[7])},c.rs,vo,so+352);
    st8(v2u{pk2(kv[8],kv[9]),pk2(kv[10],kv[11])},c.rs,hh? OOB:vo,so+704);
  }
  stamp(ws,c.b,4);
  const int t16=pi*8+rh*4+w;
  unsigned hv=WS_H+(t16>>1)*40960+cc*5120+((t16&1)*16+tk)*16;
  unsigned sv=g4==0? WS_ST+pi*8192+cc*1024+(rh*64+w*16+tk)*8 : OOB;
  c.actst = WS_ACT + ((pi*4+w)*2+(tok>>4))*55296 + hh*256 + (tok&15)*16;
  c.pk[0]=c.pk[1]=c.pk[2]=c.pk[3]=POISON;
  unsigned hv1=hv+g4*512, hv2=(g4&1)? OOB:hv+(g4>>1)*512, pfo=(lane<16? (lane*16+jq) : (pi*16+(lane&15)))*128, xo_=(t16*16+tk)*1280+cc*160+g4*16, xo2=(t16*16+tk)*1280+cc*160+g4*8;
  PIN(hv1); PIN(hv2); PIN(sv); PIN(c.actst); PIN(pfo); PIN(xo_); PIN(xo2);
  v2u pf; v4u xr0,xr1; v2u xr2;
  unsigned phq[2][10];
  { f32x16 aq[2], as2[2], rcs;
    unsigned qb[2], qw[2]; for(int a=0;a<2;a++){ qb[a]=lb+M_WQ+80*(4*q4+2*hh)+40*a+8*(qd<5?qd:4); qw[a]=qb[a]+w*12800; PIN(qb[a]); PIN(qw[a]); }
    unsigned r3=lb+M_RT+blk*10240+(8*hh+q4)*128+8*(((p4&1)<<1)|(p4>>1))+w*32;
    unsigned r4=lb+M_RT+20480+(w>>1)*5120+(8*g4+q4)*160, r4a=r4+64*(w&1)+16*p4, r4b=r4+128+8*p4; PIN(r3); PIN(r4a); PIN(r4b);
    const int tau=jl*4+w, j2= tau<96? tau/6 : tau-96, i2= tau<96? tau-j2*6 : 6;
    const unsigned d3= job? WS_WT3+j2*286720+(i2<PW? PW*jp*5+i2 : 40*i2+jp*5)*1024 : WS_END, d3s= i2<PW? PW*1024:1024;
    const int ju=lid*2+(w>>1), js=ju>>3; const unsigned d4= job? WS_WT4+(ju&7)*286720+(js<48? 16*(js%3)+js/3 : js)*5120 : WS_END;
    v4u rf[8];
    auto wopiece=[&](auto N){ constexpr int n=N, p=n%5; int s=(n/5)*4+w; unsigned so=s*32*1280+cc*160;
      if constexpr(p%4==0) dma0(ro,lb+M_WO+s*5120+p*1024,ovv[p],so); else dmaN<p*1024>(ro,ovv[p],so); };
    pipe<100,4>(
      [&](auto i){ constexpr int I=i; unsigned a=(I<20? qw[I&1]:qb[I&1])+(I<20? I/2:(I-20)/2)*1280; return trfrag(a,a+80); },
      [&](auto i,v4u A){ constexpr int I=i;
        if constexpr(I<20){ if constexpr(I<2) mfma32vv_0(as2[I&1],ONE,A); else mfma32vv(as2[I&1],ONE,A); }
        else { if constexpr(I<22) mfma32_0(aq[I&1],A,c.X[0]); else mfma32(aq[I&1],A,c.X[(I-20)/2]); } },
      [&](auto i){ constexpr int I=i;
        if constexpr(I==2){ vmwait<3>(); PIN(dummy); barrier(); }
        if constexpr(I>=3 && I<8) rf[I-3]=trfrag(r3+(I-3)*2048,r3+(I-3)*2048+512);
        if constexpr(I==8) rf[5]=trfrag(r4a,r4a+640);
        if constexpr(I==9) rf[6]=trfrag(r4a+8,r4a+648);
        if constexpr(I==10) rf[7]=trfrag(r4b,r4b+640);
        if constexpr(I>=8 && I<18 && I%2==0){ constexpr int s=(I-8)/2; st16(rf[s],c.rs,c.voff,d3+s*d3s); if constexpr(s==0) mfma32vv_0(rcs,ONE,rf[0]); else mfma32vv(rcs,ONE,rf[s]); }
        if constexpr(I==18) st16(rf[5],c.rs,c.voff,d4+(w&1)*2048);
        if constexpr(I==20) st16(rf[6],c.rs,c.voff,d4+(w&1)*2048+1024);
        if constexpr(I==22) st16(rf[7],c.rs,c.voff,(w&1)? d4+4096:WS_END);
        if constexpr(I==24){ asm volatile("" : "+v"(rcs)); float ct=rcs[0]; unsigned hi=pk2(ct,0.f); unsigned pk=pk2(ct,ct-blo(hi));
          st4(pk,c.rs,hh? OOB:(tok+32*(jp>>2))*16+(jp&3)*4,job? WS_CS3+j2*8192+i2*1024:WS_END); }
        if constexpr(I==25){ asm volatile("" : "+v"(as2[0]),"+v"(as2[1])); if(lane<32) for(int a=0;a<2;a++) *(LAS float*)(lb+M_CSX+a*512+lane*16+w*4)=as2[a][0]; }
        if constexpr(I==26) st16(PZ,c.rs,hv1,0);
        if constexpr(I==27) st16(PZ,c.rs,hv1,2048);
        if constexpr(I==28) st16(PZ,c.rs,hv2,4096);
        if constexpr(I==29) st8(v2u{PZ.x,PZ.y},c.rs,sv,0);
        if constexpr(I>=30 && I<37) act_store<I-30>(c);
        if constexpr(I==37) ld8(pf,c.rs,pfo,WS_FLAG+FK_A*256*128);
        if constexpr(I==38) asm volatile("buffer_load_dwordx4 %0, %1, %2, 0 offen" : "=v"(xr0) : "v"(xo_), "s"(rx) : "memory");
        if constexpr(I==39) asm volatile("buffer_load_dwordx4 %0, %1, %2, 0 offen offset:64" : "=v"(xr1) : "v"(xo_), "s"(rx) : "memory");
        if constexpr(I==40) asm volatile("buffer_load_dwordx2 %0, %1, %2, 0 offen offset:128" : "=v"(xr2) : "v"(xo2), "s"(rx) : "memory");
        if constexpr(I==41) barrier();
        if constexpr(I>=42 && I<92 && I%3==0) wopiece(IC<(I-42)/3>{});
        });
    vmwait<17>(); asm volatile("" : "+v"(pf),"+v"(xr0),"+v"(xr1),"+v"(xr2));
    lgk0(); barrier();
    if(tid==0) __hip_atomic_store((GAS u64*)(ws+WS_FLAG+FK_W*256*128+lid*8), c.tag, RLX);
    stamp(ws,c.b,5);
    { u64 f=((u64)pf.y<<32)|pf.x;
      if(!__all(f==c.tag)){ const GAS u64* q=(const GAS u64*)(ws+WS_FLAG+FK_A*256*128+pfo);
        for(int sp=0;sp<SPIN_CAP;++sp){ if(__all(__hip_atomic_load(q,RLX)==c.tag)) break; } } }
    v4u kp[16];
    unsigned kvo=tid<220? tid*16:OOB; PIN(kvo);
    { v4u cf[2]; for(int a=0;a<2;a++){ cf[a]=cs_frag(lb,a,lane); pinV(cf[a]); }
      nop2(); for(int a=0;a<2;a++) mfma32vv(aq[a],cf[a],MB1);
      UNROLL(8,I,ldV<0>(kp[I],c.rs,kvo,WS_KVP+(jq*16+I)*KVP_B));
      f32x16 pz[2]; v4u k0[2],k1[2]; asm volatile("s_nop 3" : "+v"(aq[0]));
      #pragma unroll
      for(int a=0;a<2;a++){ asm volatile("" : "+v"(aq[a]));
        k0[a]=pack8r(aq[a],0); k1[a]=v4u{pk2(aq[a][8],aq[a][9]),pk2(aq[a][10],aq[a][11]),0,0}; pinV(k0[a]); pinV(k1[a]); }
      nop2();
      for(int a=0;a<2;a++){ mfma32av_0(pz[a],PW_[2+a][0],k0[a]); mfma32av(pz[a],PW_[2+a][1],k1[a]); }
      UNROLL(8,I,ldV<0>(kp[8+I],c.rs,kvo,WS_KVP+(jq*16+8+I)*KVP_B));
      SB0(); asm volatile("s_nop 3" : "+v"(pz[0]),"+v"(pz[1]));
      hedgehog2(pz[0],pz[1],hh,rstd1*LOG2E,phq);
      for(int a=0;a<2;a++) for(int i=0;i<10;i++) PIN(phq[a][i]);
    }
    SB0();
    dma0(ro,lb+M_WO+(12+w)*5120,ovv[0],(12+w)*32*1280+cc*160);
    UNROLL(8,I,wopiece(IC<17+I>{}));
    stamp(ws,c.b,6);
    vmwait<9>();
    UNROLL(16,I,pinV(kp[I]));
    for(int sp=0;sp<SPIN_CAP;++sp){
      unsigned m=0; for(int i=0;i<16;i++){ m=max(m,max(kp[i].x,kp[i].z)); }
      if(!__any(m==POISON)) break;
      c.nretry+=1<<24;
      vmwait<0>(); UNROLL(16,I,ldV<0>(kp[I],c.rs,kvo,WS_KVP+(jq*16+I)*KVP_B)); vmwait<0>(); UNROLL(16,I,pinV(kp[I]));
    }
    float s8[8]={};
    for(int i=0;i<16;i++){
      s8[0]=dot2(kp[i].x,C01,s8[0]); s8[1]=dot2(kp[i].x,C10,s8[1]); s8[2]=dot2(kp[i].y,C01,s8[2]); s8[3]=dot2(kp[i].y,C10,s8[3]);
      s8[4]=dot2(kp[i].z,C01,s8[4]); s8[5]=dot2(kp[i].z,C10,s8[5]); s8[6]=dot2(kp[i].w,C01,s8[6]); s8[7]=dot2(kp[i].w,C10,s8[7]); }
    if(tid<220){ int wq=tid/55, r=tid-wq*55, qu=r/11, dp=r-qu*11; unsigned ad=lb+M_KVI+(((wq>>1)*22+2*dp)*2+(wq&1))*48+qu*8;
      *(LAS v2u*)ad=v2u{pk2(s8[0],s8[1]),pk2(s8[2],s8[3])}; *(LAS v2u*)(ad+96)=v2u{pk2(s8[4],s8[5]),pk2(s8[6],s8[7])}; }
    lgk0(); barrier();
  }
  stamp(ws,c.b,24);
  { const int rho=lane&31, h2=(rho>>2)&1, q=(rho&3)+4*(rho>>3);
    v4u ka[2][3];
    #pragma unroll
    for(int a=0;a<2;a++){
      int d = q<8 ? (a==0? 8*h2+q : 4+8*h2+q) : q<12 ? (h2? 21 : (a==0? 8+q : q-8)) : (q==12? 20:21);
      const unsigned p=lb+M_KVI+((a*22+d)*2+hh)*48;
      for(int s=0;s<3;s++) ka[a][s]=*(const LAS v4u*)(p+16*s); }
    v4u pb[2][3];
    for(int a=0;a<2;a++){ pb[a][0]=v4u{phq[a][0],phq[a][1],phq[a][2],phq[a][3]}; pb[a][1]=v4u{phq[a][4],phq[a][5],phq[a][6],phq[a][7]}; pb[a][2]=v4u{phq[a][8],phq[a][9],0,0}; for(int s=0;s<3;s++) pinV(pb[a][s]); }
    nop2();
    f32x16 o[2];
    for(int a=0;a<2;a++){ mfma32vv_0(o[a],ka[a][0],pb[a][0]); mfma32vv(o[a],ka[a][1],pb[a][1]); mfma32vv(o[a],ka[a][2],pb[a][2]); }
    mdone(o[1]); asm volatile("" : "+v"(o[0]));
    v4u oc[2]; v2u oe[2];
    for(int a=0;a<2;a++){ float r=__builtin_amdgcn_rcpf(o[a][12]+1e-6f); oc[a]=pack8(o[a],0,r); oe[a]=v2u{pk2(o[a][8]*r,o[a][9]*r),pk2(o[a][10]*r,o[a][11]*r)}; }
    st16(oc[0],c.rs,ov1,0); st16(oc[1],c.rs,ov1,768); st16(v4u{oe[0].x,oe[0].y,oe[1].x,oe[1].y},c.rs,ov2,0);
  }
  stamp(ws,c.b,7);
  unsigned wfo=lane<54? lane*32:OOB; v4u wf0,wf1;
  ldV<0>(wf0,c.rs,wfo,WS_FLAG+FK_W*256*128); ldV<16>(wf1,c.rs,wfo,WS_FLAG+FK_W*256*128);
  vmwait<5>(); barrier();
  stamp(ws,c.b,15);
  c.src3 = WS_WT3 + pj*286720 + w*5120;
  c.src4 = WS_WT4 + cc*286720 + w*5120;
  c.wlo = WS_WT4 + cc*286720 + 240*1024 + kh*5120;
  v4u hr0,hr1; v2u hr2;
  { v4u WA[12][5];
    unsigned wb[2][3]; const unsigned rb=lb+M_WO+160*(16*(g4>>1)+4*(g4&1)+q4);
    wb[0][0]=rb+16*p4+8*(p4>>1); wb[0][1]=rb+16*p4+8*(1^(p4>>1)); wb[0][2]=rb+128+8*p4;
    for(int t=0;t<3;t++){ wb[1][t]=wb[0][t]+61440; PIN(wb[0][t]); PIN(wb[1][t]); }
    v4u WB[4][5];
    #pragma unroll
    for(int s=0;s<17;s++){
      #pragma unroll
      for(int u=0;u<5;u++){
        if(s<12){ unsigned a=wb[0][u<4? u&1:2]+s*5120+(u<4? 64*(u>>1):0); WA[s][u]=trfrag(a,a+1280); }
        else if(s<16){ unsigned a=wb[1][u<4? u&1:2]+(s-12)*5120+(u<4? 64*(u>>1):0); WB[s-12][u]=trfrag(a,a+1280); } }
      SB0();
      #pragma unroll
      for(int u=0;u<5;u++){ if(s>=1 && s<13) pinA(WA[s-1][u]); else if(s>=13) pinV(WB[s-13][u]); }
      SB0(); }
    barrier();
    stamp(ws,c.b,16);
    { const unsigned tl=(unsigned)c.tag, thi=(unsigned)(c.tag>>32);
      vmwait<0>(); pinV(wf0); pinV(wf1);
      for(int sp=0;sp<SPIN_CAP;++sp){
        bool ok = lane>=54 || (wf0.x==tl && wf0.y==thi && wf0.z==tl && wf0.w==thi && wf1.x==tl && wf1.y==thi && wf1.z==tl && wf1.w==thi);
        if(__all(ok)) break;
        c.nretry+=1<<24;
        ldV<0>(wf0,c.rs,wfo,WS_FLAG+FK_W*256*128); ldV<16>(wf1,c.rs,wfo,WS_FLAG+FK_W*256*128);
        vmwait<0>(); pinV(wf0); pinV(wf1);
      } }
    v4u O[20]; const unsigned oo=WS_O+t16*20480;
    UNROLL(20,I,ldV<(I%4)*1024>(O[I],c.rs,c.voff,oo+(I/4)*4096));
    stamp(ws,c.b,17);
    f32x4 a2[5];
    v4u WC[4][5];
    auto p2step=[&](auto i){ constexpr int I=i, s=I/5, u=I%5;
      if constexpr(u==0 && s%4==0){
        vmwait<16-s+s>(); pinV(O[s]); pinV(O[s+1]); pinV(O[s+2]); pinV(O[s+3]);
        if(__builtin_expect(__any(max(max(O[s].x,O[s+1].x),max(O[s+2].x,O[s+3].x))==POISON),0)){
          for(int sp=0;sp<SPIN_CAP;++sp){ vmwait<0>();
            UNROLL(20-s,J,ldV<((s+J)%4)*1024>(O[s+J],c.rs,c.voff,oo+((s+J)/4)*4096));
            vmwait<16-s>(); pinV(O[s]); pinV(O[s+1]); pinV(O[s+2]); pinV(O[s+3]); c.nretry+=1<<24;
            if(!__any(max(max(O[s].x,O[s+1].x),max(O[s+2].x,O[s+3].x))==POISON)) break; }
          if constexpr(s>0) vmwait<0>();
        } }
      if constexpr(s==0) mfma16a_0(a2[u],WA[0][u],O[0]);
      else if constexpr(s<12) mfma16a(a2[u],WA[s<12?s:0][u],O[s]);
      else if constexpr(s<16) mfma16(a2[u],WB[(s-12)&3][u],O[s]);
      else mfma16(a2[u],WC[(s-16)&3][u],O[s]);
      SB0();
      if constexpr(u==1) issue_piece<s>(c);
      if constexpr(I>=20 && I<60 && I%2==0){ constexpr int n=(I-20)/2, s2=n/5, u2=n%5; unsigned a=wb[1][u2<4? u2&1:2]+(4+s2)*5120+(u2<4? 64*(u2>>1):0); WC[s2][u2]=trfrag(a,a+1280); }
      SB0(); };
    UNROLL(100,I,p2step(IC<I>{}));
    asm volatile("s_nop 7" : "+v"(a2[0]),"+v"(a2[1]),"+v"(a2[2]),"+v"(a2[3]),"+v"(a2[4]));
    stamp(ws,c.b,26);
    const bool sw=g4>=2;
    float hv_[20];
    for(int r=0;r<4;r++){ hv_[r]= sw? a2[1][r]:a2[0][r]; hv_[4+r]= sw? a2[0][r]:a2[1][r]; hv_[8+r]= sw? a2[3][r]:a2[2][r]; hv_[12+r]= sw? a2[2][r]:a2[3][r]; hv_[16+r]=a2[4][r]; }
    hr0=v4u{pk2(hv_[0]+blo(xr0.x),hv_[1]+bhi(xr0.x)),pk2(hv_[2]+blo(xr0.y),hv_[3]+bhi(xr0.y)),pk2(hv_[4]+blo(xr0.z),hv_[5]+bhi(xr0.z)),pk2(hv_[6]+blo(xr0.w),hv_[7]+bhi(xr0.w))};
    hr1=v4u{pk2(hv_[8]+blo(xr1.x),hv_[9]+bhi(xr1.x)),pk2(hv_[10]+blo(xr1.y),hv_[11]+bhi(xr1.y)),pk2(hv_[12]+blo(xr1.z),hv_[13]+bhi(xr1.z)),pk2(hv_[14]+blo(xr1.w),hv_[15]+bhi(xr1.w))};
    hr2=v2u{pk2(hv_[16]+blo(xr2.x),hv_[17]+bhi(xr2.x)),pk2(hv_[18]+blo(xr2.y),hv_[19]+bhi(xr2.y))};
    st16(hr0,c.rs,hv1,0); st16(hr1,c.rs,hv1,2048);
    { auto sx=__builtin_amdgcn_permlane16_swap(hr2.x,hr2.x,false,false); auto sy=__builtin_amdgcn_permlane16_swap(hr2.y,hr2.y,false,false);
      st16(v4u{sx[0],sy[0],sx[1],sy[1]},c.rs,hv2,4096); }
    unsigned hw[10]={hr0.x,hr0.y,hr0.z,hr0.w,hr1.x,hr1.y,hr1.z,hr1.w,hr2.x,hr2.y};
    float s1=0.f,s1b=0.f; for(int i=0;i<5;i++){ s1=dot2(hw[2*i],C11,s1); s1b=dot2(hw[2*i+1],C11,s1b); }
    s1=xh_add(x16_add(s1+s1b))*(1.f/80.f);
    f32x2 s2={0.f,0.f}; for(int i=0;i<10;i++){ f32x2 d=f32x2{blo(hw[i]),bhi(hw[i])}-s1; s2=__builtin_elementwise_fma(d,d,s2); }
    float s2t=xh_add(x16_add(s2[0]+s2[1]));
    st8(v2u{__float_as_uint(s1),__float_as_uint(s2t)},c.rs,sv,0);
    stamp(ws,c.b,18);
  }
  for(int i=0;i<3;i++){ c.lp[i]=lb + lane*16 + i*65536; c.lq[i]=lb + lane*16 + i*61440 + kh*5120; asm volatile("" : "+v"(c.lp[i]), "+v"(c.lq[i])); }
  barrier();
  stamp(ws,c.b,25);
  UNROLL(20,I,issue_piece<20+I>(c));
  UNROLL(7,I,ldA<(I%4)*1024>(c.CS[I], c.rs, c.voff, WS_CS3 + pj*8192 + (I/4)*4096));
  stamp(ws,c.b,19);
  v2u stt[4];
  c.xo = WS_H + (pi*4+w)*40960;
  for(int sp=0;sp<SPIN_CAP;++sp){
    for(int q=0;q<4;q++) ld8(stt[q],c.rs,(unsigned)((4*hh+q)*1024+(32*w+tok)*8),WS_ST+pi*8192);
    UNROLL(XPRE,I,xload<I>(c));
    vmwait<0>(); UNROLL(XPRE,I,pinA(c.X[I]));
    asm volatile("" : "+v"(stt[0]),"+v"(stt[1]),"+v"(stt[2]),"+v"(stt[3]));
    unsigned m=max(max(stt[0].x,stt[1].x),max(stt[2].x,stt[3].x));
    for(int i=0;i<XPRE;i++) m=max(m,c.X[i].x);
    if(!__any(m==POISON)) break;
    c.nretry+=1<<24;
  }
  barrier();
  for(int r=0;r<RA;r++) c.A[r]=ldsA(c,r);
  { float m0=__uint_as_float(stt[0].x),m1=__uint_as_float(stt[1].x),m2=__uint_as_float(stt[2].x),m3=__uint_as_float(stt[3].x);
    float mean=xh_add((m0+m1)+(m2+m3))*0.125f;
    float q=(__uint_as_float(stt[0].y)+__uint_as_float(stt[1].y))+(__uint_as_float(stt[2].y)+__uint_as_float(stt[3].y));
    q += 80.f*(((m0-mean)*(m0-mean)+(m1-mean)*(m1-mean))+((m2-mean)*(m2-mean)+(m3-mean)*(m3-mean)));
    q = xh_add(q);
    float var=q*(1.f/640.f)+1e-5f; c.rstd=__builtin_amdgcn_rsqf(var); c.sd=var*c.rstd;
    unsigned mb=pk2(-mean,-mean); c.MB=v4u{mb,mb,mb,mb}; pinV(c.MB); PIN(c.rstd); PIN(c.sd);
  }
  c.actld = WS_ACT + (pi*8+rh*4+2*th)*55296 + kh*1024;
  c.g0=c.g1=c.t0=c.t1=c.e0=c.e1=c.v0=c.v1=0.f;
  SB0();
  stamp(ws,c.b,20);
  UNROLL(GN,I,gapx<I>(c));
  stamp(ws,c.b,21);
  asm volatile("s_nop 7" ::: "memory");
  barrier();
  { LAS f32x4* xw=(LAS f32x4*)(lb + w*5120 + lane*16); const LAS f32x4* xq=(const LAS f32x4*)(lb + (w^1)*5120 + lane*16);
    for(int u=0;u<5;u++) xw[u*64] = kh? c.acc4[0][u] : c.acc4[1][u];
    lgk0(); barrier();
    f32x4 r[5];
    for(int u=0;u<5;u++) r[u] = (kh? c.acc4[1][u] : c.acc4[0][u]) + xq[u*64];
    v4u o0,o1; v2u o2;
    o0.x=pk2(r[0][0]+blo(hr0.x),r[0][1]+bhi(hr0.x)); o0.y=pk2(r[0][2]+blo(hr0.y),r[0][3]+bhi(hr0.y));
    o0.z=pk2(r[1][0]+blo(hr0.z),r[1][1]+bhi(hr0.z)); o0.w=pk2(r[1][2]+blo(hr0.w),r[1][3]+bhi(hr0.w));
    o1.x=pk2(r[2][0]+blo(hr1.x),r[2][1]+bhi(hr1.x)); o1.y=pk2(r[2][2]+blo(hr1.y),r[2][3]+bhi(hr1.y));
    o1.z=pk2(r[3][0]+blo(hr1.z),r[3][1]+bhi(hr1.z)); o1.w=pk2(r[3][2]+blo(hr1.w),r[3][3]+bhi(hr1.w));
    o2.x=pk2(r[4][0]+blo(hr2.x),r[4][1]+bhi(hr2.x)); o2.y=pk2(r[4][2]+blo(hr2.y),r[4][3]+bhi(hr2.y));
    u16* op = ar.out + (t16*16+tk)*D + cc*80;
    *(v4u*)(op+8*g4)=o0; *(v4u*)(op+32+8*g4)=o1; *(v2u*)(op+64+4*g4)=o2;
  }
  if(c.b==0 && tid==0) __hip_atomic_store((GAS u64*)(ws+WS_EPOCH), c.tag, RLX);
  asm volatile(".Lhg_code_end:");
  stamp(ws,c.b,22);
}

extern "C" void kernel_launch(void* const* d_in, const int* in_sizes, int n_in,
                              void* d_out, int out_size, void* d_ws, size_t ws_size,
                              hipStream_t stream) {
  Args a; a.x=(const u16*)d_in[0]; a.wqkv=(const u16*)d_in[1]; a.wphi=(const u16*)d_in[2]; a.wo=(const u16*)d_in[3];
  a.wg=(const u16*)d_in[5]; a.wu=(const u16*)d_in[6]; a.wd=(const u16*)d_in[7]; a.out=(u16*)d_out; a.ws=(char*)d_ws;
  hipFuncSetAttribute((const void*)blk_k, hipFuncAttributeMaxDynamicSharedMemorySize, 163840);
  blk_k<<<256,256,163840,stream>>>(a);
}
```

```cpp
#define XPV 12
#define NPRE 30
#define WLD 310
#define TG1 35
#define TG3 38
#define TG4 37
#define TOP 70
#define QAL 420
#define PL0 200
#define PL1 262
#define PL2 36
#define PL3 60
#define XROT 1
#define AROT 0
constexpr int PW = 2;
constexpr int G3 = 280, GE = 288, GN = GE+135, NSTG = 26, RA = 6, NA = 8;
constexpr int G1 = 40*PW, EP0 = G1+4, EPL = 7, XPRE = XPV, XVER = 3;
enum { OP_NONE=0, OP_XL, OP_DMA, OP_ST, OP_WL, OP_AL, OP_PL, OP_CS };
constexpr bool is_sync(int g){ return g<G3 ? (g%20==0 && g>=G1) : (g>=GE && g<GE+120 && (g-GE)%20==0); }
constexpr int sync_stage(int g){ return g<G3 ? g/20 : 14+(g-GE)/10; }
constexpr int sync_need(int g){ int m = g<G3 ? g/20+1 : 14+(g-GE)/10+2; return m<NSTG? m : NSTG-1; }
constexpr int ep_start(int t){ int g=EP0; for(int i=1;i<=t;i++){ g+=4*EPL; if(i>=PW && g<40*i+44) g=40*i+44; } return g; }
constexpr int PLG[4] = {PL0, PL1, GE+PL2, GE+PL3};
constexpr int PCG[4] = {PL0+14, PL1+14, GE+PL2+14, GE+PL3+14};
constexpr int FSG[4] = {160, 220, GE, GE+20};
constexpr int FST[4] = {1, 3, 5, 6};
struct Sched { int op[GN+1]; int ix[GN+1]; int cnt[GN+2]; int dmag[NSTG]; int stg[7]; int xg[40]; int ag[54]; int wg[15]; int cg[7]; int ept[GN+1]; };
constexpr int al_kind(int i){ return i<48 ? i/16 : 3; }
constexpr int cs_use(int t){ return t<PW ? G1-PW+t : 40*t+39; }
constexpr Sched make_sched(){
  Sched s{};
  for(int g=0;g<=GN;g++) s.ept[g]=-1;
  for(int t=0;t<6;t++){ int g0=ep_start(t); for(int e=0;e<4*EPL;e++) s.ept[g0+e]=t*4*EPL+e; int g=g0+4*EPL; s.op[g]=OP_ST; s.ix[g]=t; s.stg[t]=g; }
  s.op[GE-1]=OP_ST; s.ix[GE-1]=6; s.stg[6]=GE-1;
  for(int k=0;k<4;k++){ s.op[PLG[k]]=OP_PL; s.ix[PLG[k]]=k; }
  for(int m=0;m<NSTG;m++) s.dmag[m]=-1;
  for(int i=0;i<XPRE;i++) s.xg[i]=-1;
  int ddl[NSTG]={};
  for(int m=0;m<NSTG;m++) for(int q=0;q<GN;q++) if(is_sync(q) && sync_need(q)>=m){ ddl[m]=q; break; }
  int nx=XPRE, nd=NPRE, na=0, nw=0, nc=0, cur=0, now=0, busy=8*TOP;
  for(int g=0; g<GN; g++){
    if(is_sync(g)) cur=sync_stage(g);
    now += g<EP0 ? TG1 : g<G3 ? TG3 : g<GE ? 40 : TG4;
    if(busy<now) busy=now;
    if(s.op[g]!=OP_NONE){ busy+=34; continue; }
    if(busy-now>QAL) continue;
    int pick=OP_NONE;
    if(nx<40) pick=OP_XL;
    else {
      int dd=1<<20, da=1<<20, dc=1<<20;
      if(nd<NSTG*5 && nd/5<=cur+7) dd=ddl[nd/5];
      if(na<54 && g>=PCG[al_kind(na)]) da=GE+(na/2)*5;
      if(nc<7) dc=cs_use(nc);
      int dw = (nw<15 && g>=60) ? WLD : (1<<20);
      int mn = dd<da? dd:da; mn = mn<dc? mn:dc; mn = mn<dw? mn:dw;
      if(mn<(1<<20)) pick = mn==dc? OP_CS : mn==da? OP_AL : mn==dd? OP_DMA : OP_WL;
    }
    if(pick==OP_NONE) continue;
    s.op[g]=pick; busy+=TOP;
    if(pick==OP_XL){ s.ix[g]=nx; s.xg[nx]=g; nx++; }
    if(pick==OP_DMA){ s.ix[g]=nd; s.dmag[nd/5]=g; nd++; }
    if(pick==OP_AL){ s.ix[g]=na; s.ag[na]=g; na++; }
    if(pick==OP_WL){ s.ix[g]=nw; s.wg[nw]=g; nw++; }
    if(pick==OP_CS){ s.ix[g]=nc; s.cg[nc]=g; nc++; }
  }
  s.cnt[0]=0;
  for(int g=0;g<=GN;g++) s.cnt[g+1]=s.cnt[g]+(s.op[g]!=OP_NONE);
  return s;
}
constexpr Sched SC = make_sched();
constexpr bool sched_ok(){
  if(NPRE%5 || NPRE<25 || NPRE>35) return false;
  for(int g=0;g<GN;g++) if(is_sync(g) && SC.dmag[sync_need(g)]>=g) return false;
  for(int m=NPRE/5;m<NSTG;m++) if(SC.dmag[m]<0) return false;
  for(int s=XPRE;s<40;s++) if(SC.xg[s]>=PW*s-2) return false;
  for(int i=0;i<54;i++) if(SC.ag[i]<=0 || SC.ag[i]>=GE+(i/2)*5-1) return false;
  for(int i=0;i<15;i++) if(SC.wg[i]<=0 || SC.wg[i]>=SC.ag[49]) return false;
  for(int i=0;i<7;i++) if(SC.cg[i]<=0 || SC.cg[i]>=cs_use(i)) return false;
  for(int k=0;k<4;k++) if(SC.stg[FST[k]]>=FSG[k] || !is_sync(FSG[k]) || SC.op[PLG[k]]!=OP_PL) return false;
  for(int t=0;t<7;t++) if(SC.op[SC.stg[t]]!=OP_ST) return false;
  if(SC.stg[5]>=G3) return false;
  return true;
}
static_assert(sched_ok(), "schedule");
constexpr int after(int g, int g0){ return SC.cnt[g]-SC.cnt[g0+1]; }

struct Ctx {
  v4u rs; char* ws; unsigned lp[3], lq[3]; unsigned voff; int lane, w, b, pi, pj; v2u tag;
  unsigned src3, src4, actst, actld, av[2], xo, wlo, pollv, flagv, rot, xmax;
  v4u X[40]; v4u CS[7]; v4u WL[15]; v4u MB; v4u A[NA];
  f32x16 acc[7]; f32x4 acc4[2][5]; v4u B[54]; v2u pf;
  float c1r, c2r, var; float t0,t1,e0,e1,v0,v1; unsigned pk[4];
  unsigned nretry;
};
DI v4u ldsA(const Ctx& c, int a){
  if(a<G3) return *(const LAS v4u*)(c.lp[((a+100)%160)/64] + (((a+100)%160)%64)*1024);
  int q=a-G3; int f=(G3 + 100 + (q/5)*10 + q%5)%160;
  return *(const LAS v4u*)(c.lq[f/60] + (f%60)*1024);
}
template<int M> DI void issue_piece(Ctx& c){
  constexpr int stg=M/5, p=M%5, pb = p<4 ? 0 : 4;
  unsigned lds = ((stg+5)%8)*20480 + pb*1024 + c.w*5120;
  unsigned so = (stg<14 ? c.src3 + stg*20480 : c.src4 + (stg-14)*20480) + pb*1024;
  if constexpr(p==pb) dma1(c.rs, lds, c.voff, so);
  else asm volatile("buffer_load_dwordx4 %0, %1, %2 offen offset:%3 sc1 lds" :: "v"(c.voff), "s"(c.rs), "s"(so), "n"(p*1024) : "memory");
}
constexpr float GC1 = -2.302208198f, GC2 = -0.1029432397f;
#define PIN(x) asm volatile("" : "+v"(x))
template<int T,int P,int STEP> DI void epi7(Ctx& c){
  constexpr int q=2*P;
  if constexpr(STEP==0){ c.t0=c.acc[T][q]*c.acc[T][q]; PIN(c.t0); c.t1=c.acc[T][q+1]*c.acc[T][q+1]; PIN(c.t1); c.v0=c.acc[T][q]*c.acc[T][q+8]; PIN(c.v0); }
  if constexpr(STEP==1){ c.t0=__builtin_fmaf(c.t0,c.c2r,c.c1r); PIN(c.t0); c.t1=__builtin_fmaf(c.t1,c.c2r,c.c1r); PIN(c.t1); c.v1=c.acc[T][q+1]*c.acc[T][q+9]; PIN(c.v1); }
  if constexpr(STEP==2){ c.t0*=c.acc[T][q]; PIN(c.t0); c.t1*=c.acc[T][q+1]; PIN(c.t1); }
  if constexpr(STEP==3){ c.e0=__builtin_amdgcn_exp2f(c.t0); PIN(c.e0); c.e1=__builtin_amdgcn_exp2f(c.t1); PIN(c.e1); }
  if constexpr(STEP==4){ c.e0=__builtin_fmaf(c.e0,c.var,c.var); PIN(c.e0); c.e1=__builtin_fmaf(c.e1,c.var,c.var); PIN(c.e1); }
  if constexpr(STEP==5){ c.e0=__builtin_amdgcn_rcpf(c.e0); PIN(c.e0); c.e1=__builtin_amdgcn_rcpf(c.e1); PIN(c.e1); }
  if constexpr(STEP==6){ c.v0*=c.e0; PIN(c.v0); c.v1*=c.e1; PIN(c.v1); c.pk[P]=pk2(c.v0,c.v1); PIN(c.pk[P]); }
}
struct Epi { f32x2 t[4], v[4]; };
template<int STEP> DI void epiw(Ctx& c, Epi& e){
  for(int i=0;i<4;i++){ const f32x2 a={c.acc[6][2*i],c.acc[6][2*i+1]};
    if constexpr(STEP==0){ e.t[i]=a*a; e.v[i]=a*f32x2{c.acc[6][2*i+8],c.acc[6][2*i+9]}; }
    if constexpr(STEP==1) e.t[i]=__builtin_elementwise_fma(e.t[i],f32x2{c.c2r,c.c2r},f32x2{c.c1r,c.c1r});
    if constexpr(STEP==2) e.t[i]*=a;
    if constexpr(STEP==3) e.t[i]=f32x2{__builtin_amdgcn_exp2f(e.t[i][0]),__builtin_amdgcn_exp2f(e.t[i][1])};
    if constexpr(STEP==4) e.t[i]=__builtin_elementwise_fma(e.t[i],f32x2{c.var,c.var},f32x2{c.var,c.var});
    if constexpr(STEP==5) e.t[i]=f32x2{__builtin_amdgcn_rcpf(e.t[i][0]),__builtin_amdgcn_rcpf(e.t[i][1])};
    if constexpr(STEP==6) e.v[i]*=e.t[i];
    if constexpr(STEP==7) c.pk[i]=pk2(e.v[i][0],e.v[i][1]); }
  if constexpr(STEP<7) asm volatile("" : "+v"(e.t[0]),"+v"(e.t[1]),"+v"(e.t[2]),"+v"(e.t[3]),"+v"(e.v[0]),"+v"(e.v[1]),"+v"(e.v[2]),"+v"(e.v[3]));
}
template<int T> DI void act_store(Ctx& c){
  unsigned so = T<6 ? (16*(T>>1))*1024 + (T&1)*512 + c.pj*1024 : 48*1024 + (c.pj>>1)*1024 + (c.pj&1)*512;
  if(T==6 && c.pj>=12) so = WS_END;
  st16(v4u{c.pk[0],c.pk[1],c.pk[2],c.pk[3]}, c.rs, c.actst, so);
}
template<int ix> DI void xload(Ctx& c){
  if constexpr(XROT){ constexpr int e=ix%5; ldA<(e&3)*1024>(c.X[ix], c.rs, c.voff, c.xo + ((ix/5+c.rot)&7)*5120 + (e&4)*1024); }
  else ldA<(ix%4)*1024>(c.X[ix], c.rs, c.voff, c.xo + (ix/4)*4096); }
template<int ix> DI void aload(Ctx& c){ constexpr int m=ix/2;
  if constexpr(AROT && m<24) ldV<0>(c.B[ix], c.rs, c.av[ix&1], c.actld + (m/8)*16384+((m+c.rot)&7)*2048);
  else ldV<(m&1)*2048>(c.B[ix], c.rs, c.av[ix&1], c.actld + (m/2)*4096); }
template<int g> DI void vmem_op(Ctx& c){
  constexpr int op=SC.op[g], ix=SC.ix[g];
  if constexpr(op==OP_XL) xload<ix>(c);
  if constexpr(op==OP_DMA) issue_piece<ix>(c);
  if constexpr(op==OP_ST) act_store<ix>(c);
  if constexpr(op==OP_WL) ldA<((ix%5)&3)*1024>(c.WL[ix], c.rs, c.voff, c.wlo + (ix/5)*10240 + ((ix%5)&4)*1024);
  if constexpr(op==OP_CS) ldA<(ix%4)*1024>(c.CS[ix], c.rs, c.voff, WS_CS3 + c.pj*8192 + (ix/4)*4096);
  if constexpr(op==OP_AL) aload<ix>(c);
  if constexpr(op==OP_PL) ld8(c.pf, c.rs, c.pollv, WS_FLAG+(FK_D0+ix)*256*128);
}
constexpr unsigned POISON = 0xffffffffu;
DI void useA(v4u a, v4u b, v4u c, v4u d){ asm volatile("" :: "v"(a),"v"(b),"v"(c),"v"(d)); }
template<int g> DI void gapx(Ctx& c, Epi& ew){
  if constexpr(is_sync(g)){
    constexpr int m=sync_need(g);
    constexpr int n1 = SC.dmag[m]>=0 ? after(g,SC.dmag[m]) : 63;
    constexpr int k = g==FSG[0]?0 : g==FSG[1]?1 : g==FSG[2]?2 : g==FSG[3]?3 : -1;
    constexpr int n2 = k>=0 ? after(g,SC.stg[FST[k<0?0:k]]) : 63;
    if constexpr((n1<n2?n1:n2)<63) vmwait<(n1<n2?n1:n2)>();
    barrier();
    if constexpr(k>=0){ if(c.w==k) st8(c.tag, c.rs, c.flagv, WS_FLAG+(FK_D0+k)*256*128); }
    SB0();
  }
  if constexpr(g<G3){
    constexpr int T = g<G1 ? g%PW : g/40, s = g<G1 ? g/PW : g%40;
    if constexpr(g%4==0) useA(c.A[g%NA],c.A[(g+1)%NA],c.A[(g+2)%NA],c.A[(g+3)%NA]);
    if constexpr(s==0) mfma32_0(c.acc[T], c.A[g%NA], c.X[s]); else mfma32(c.acc[T], c.A[g%NA], c.X[s]);
    if constexpr(s==39){ vmwait<after(g,SC.cg[T])>(); mfma32av(c.acc[T], c.CS[T], c.MB); }
    SB0();
    c.A[(g+RA)%NA] = ldsA(c, g+RA);
    vmem_op<g>(c);
    if constexpr(g<G1 && s>=XVER && T==0){ pinA(c.X[s]); c.e0=__uint_as_float(c.X[s].x); PIN(c.e0); }
    if constexpr(g<G1 && s>=XVER && T==1){ c.xmax=max(c.xmax,__float_as_uint(c.e0)); PIN(c.xmax); }
    if constexpr(g<G1 && T==PW-1 && s+1>=XVER && s+1<40){ if constexpr(s+1<XPRE) vmwait<XPRE-2-s+SC.cnt[g+1]>(); else vmwait<after(g+1,SC.xg[s+1])>(); }
    if constexpr(SC.ept[g]>=0){ constexpr int e=SC.ept[g]; epi7<e/(4*EPL),(e/EPL)%4,e%EPL>(c); }
  } else if constexpr(g<GE){
    if constexpr(g==G3) asm volatile("s_nop 9" : "+v"(c.acc[6]));
    epiw<g-G3>(c,ew);
    vmem_op<g>(c);
  } else {
    constexpr int q=g-GE, m=q/5, u=q%5, a=G3+q;
    if constexpr(q<120 && q%4==0) useA(c.A[a%NA],c.A[(a+1)%NA],c.A[(a+2)%NA],c.A[(a+3)%NA]);
    if constexpr(q<120){
      if constexpr(m==0) mfma16_0(c.acc4[1][u], c.A[a%NA], c.B[1]); else mfma16(c.acc4[1][u], c.A[a%NA], c.B[2*m+1]);
      SB0();
      vmem_op<g>(c);
      SB0();
      if constexpr(m==0) mfma16_0(c.acc4[0][u], c.A[a%NA], c.B[0]); else mfma16(c.acc4[0][u], c.A[a%NA], c.B[2*m]);
    } else { mfma16a(c.acc4[1][u], c.WL[q-120], c.B[2*m+1]); SB0(); vmem_op<g>(c); SB0(); mfma16a(c.acc4[0][u], c.WL[q-120], c.B[2*m]); }
    SB0();
    if constexpr(q+RA<120) c.A[(a+RA)%NA] = ldsA(c, a+RA);
  }
  if constexpr(g>=GE-1 && (g+1-GE)%5==0 && g+1<GN) vmwait<after(g+1,SC.ag[2*((g+1-GE)/5)+1])>();
  { constexpr int k = g+1==PCG[0]?0 : g+1==PCG[1]?1 : g+1==PCG[2]?2 : g+1==PCG[3]?3 : -1;
    if constexpr(k>=0){
      vmwait<after(g+1,PLG[k])>(); asm volatile("" : "+v"(c.pf));
      if(__builtin_expect(__any(((c.pf.x^c.tag.x)|(c.pf.y^c.tag.y))!=0),0)){ c.nretry+=256<<(4*k);
        for(int sp=0;sp<SPIN_CAP;++sp){ ld8(c.pf, c.rs, c.pollv, WS_FLAG+(FK_D0+k)*256*128); vmwait<0>(); asm volatile("" : "+v"(c.pf)); if(!__any(((c.pf.x^c.tag.x)|(c.pf.y^c.tag.y))!=0)) break; } }
    } }
  SB0();
}
#define MAPM 4
#define OL0 10
#define REPS 1
#define RP0 8
#define OROT 1
#define PZC 0
#define ROT 1
constexpr int REDO_CAP=64;
template<int I> using IC = std::integral_constant<int,I>;
template<int N,int DP,class RD,class MM,class SD> DI void pipe(RD rd, MM mm, SD sd){
  v4u A[DP+1];
  [&]<int... I>(std::integer_sequence<int,I...>) AI { ((A[I]=rd(IC<I>{})), ...); }(std::make_integer_sequence<int,DP>{});
  SB0();
  auto step=[&](auto i) AI { constexpr int I=i; if constexpr(I%2==0 && I+1<N) asm volatile("" :: "v"(A[I%(DP+1)]),"v"(A[(I+1)%(DP+1)])); mm(i,A[I%(DP+1)]); SB0(); sd(i); if constexpr(I+DP<N) A[(I+DP)%(DP+1)]=rd(IC<I+DP>{}); SB0(); };
  [&]<int... I>(std::integer_sequence<int,I...>) AI { (step(IC<I>{}), ...); }(std::make_integer_sequence<int,N>{});
}
DI void mfma32vv_0(f32x16& c, v4u a, v4u x){ asm volatile("v_mfma_f32_32x32x16_bf16 %0, %1, %2, 0" : "=&v"(c) : "v"(a), "v"(x)); }
DI void mfma32vv(f32x16& c, v4u a, v4u x){ asm volatile("v_mfma_f32_32x32x16_bf16 %0, %1, %2, %0" : "+v"(c) : "v"(a), "v"(x)); }
DI void mfma32A_0(f32x16& c, v4u a, v4u x){ asm volatile("v_mfma_f32_32x32x16_bf16 %0, %1, %2, 0" : "=a"(c) : "v"(a), "v"(x)); }
DI void mfma32A(f32x16& c, v4u a, v4u x){ asm volatile("v_mfma_f32_32x32x16_bf16 %0, %1, %2, %0" : "+a"(c) : "v"(a), "v"(x)); }
DI void mfma32av_0(f32x16& c, v4u a, v4u x){ asm volatile("v_mfma_f32_32x32x16_bf16 %0, %1, %2, 0" : "=&v"(c) : "a"(a), "v"(x)); }
DI void mfma16a_0(f32x4& c, v4u a, v4u x){ asm volatile("v_mfma_f32_16x16x32_bf16 %0, %1, %2, 0" : "=&v"(c) : "a"(a), "v"(x)); }
DI void nop2(){ asm volatile("s_nop 1" ::: "memory"); }
DI void mdone(f32x16& c){ asm volatile("s_nop 11" : "+v"(c)); }
#define PINS(x) asm volatile("" : "+s"(x))
DI float max3(float a,float b,float c){ float r; asm("v_max3_f32 %0, %1, %2, %3" : "=v"(r) : "v"(a),"v"(b),"v"(c)); return r; }
DI float min3(float a,float b,float c){ float r; asm("v_min3_f32 %0, %1, %2, %3" : "=v"(r) : "v"(a),"v"(b),"v"(c)); return r; }
DI f32x2 xh(float v){ auto r=__builtin_amdgcn_permlane32_swap(__float_as_uint(v),__float_as_uint(v),false,false); return f32x2{__uint_as_float(r[0]),__uint_as_float(r[1])}; }
template<class H> DI void hedgehog2(f32x16 p, f32x16 r, bool hh, float sc, unsigned (&o)[2][10], H hook){
  f32x16 z[2]={p,r};
  float a3[2][3], i3[2][3], mx[2], mn[2];
  for(int a=0;a<2;a++){ float x8=z[a][8],x9=z[a][9],xa=z[a][10],xb=z[a][11]; PIN(xa); PIN(xb); z[a][8]=hh? xa:x8; z[a][9]=hh? xb:x9; }
  for(int a=0;a<2;a++) for(int k=0;k<2;k++){ a3[a][k]=max3(z[a][3*k],z[a][3*k+1],z[a][3*k+2]); i3[a][k]=min3(z[a][3*k],z[a][3*k+1],z[a][3*k+2]); }
  SB0();
  for(int a=0;a<2;a++){ a3[a][2]=max3(z[a][6],z[a][7],z[a][8]); i3[a][2]=min3(z[a][6],z[a][7],z[a][8]); }
  for(int a=0;a<2;a++){ mx[a]=max3(a3[a][0],a3[a][1],z[a][9]); mn[a]=min3(i3[a][0],i3[a][1],z[a][9]); }
  SB0();
  for(int a=0;a<2;a++){ mx[a]=max3(mx[a],a3[a][2],a3[a][2]); mn[a]=min3(mn[a],i3[a][2],i3[a][2]); }
  SB0();
  f32x2 sx[2], sn_[2];
  for(int a=0;a<2;a++){ sx[a]=xh(mx[a]); sn_[a]=xh(mn[a]); }
  SB0();
  for(int a=0;a<2;a++){ mx[a]=max3(sx[a][0],sx[a][1],sx[a][1]); mn[a]=min3(sn_[a][0],sn_[a][1],sn_[a][1]); }
  SB0();
  for(int a=0;a<2;a++){ mx[a]*=-sc; mn[a]*=sc; }
  SB0(); hook(0); SB0();
  f32x2 ep[2][5], en[2][5];
  for(int a=0;a<2;a++) for(int i=0;i<5;i++){ f32x2 v={z[a][2*i],z[a][2*i+1]};
      ep[a][i]=__builtin_elementwise_fma(v,f32x2{sc,sc},f32x2{mx[a],mx[a]}); en[a][i]=__builtin_elementwise_fma(v,f32x2{-sc,-sc},f32x2{mn[a],mn[a]}); }
  SB0();
  for(int a=0;a<2;a++){
    for(int i=0;i<5;i++){ ep[a][i]=f32x2{__builtin_amdgcn_exp2f(ep[a][i][0]),__builtin_amdgcn_exp2f(ep[a][i][1])}; en[a][i]=f32x2{__builtin_amdgcn_exp2f(en[a][i][0]),__builtin_amdgcn_exp2f(en[a][i][1])}; }
    SB0(); hook(1+a); SB0(); }
  f32x2 s0[4], s1[4];
  for(int a=0;a<2;a++){ s0[a]=ep[a][0]+ep[a][1]; s1[a]=ep[a][2]+ep[a][3]; s0[2+a]=en[a][0]+en[a][1]; s1[2+a]=en[a][2]+en[a][3]; }
  SB0();
  for(int a=0;a<2;a++){ s0[a]+=ep[a][4]; s0[2+a]+=en[a][4]; }
  SB0();
  for(int k=0;k<4;k++) s0[k]+=s1[k];
  SB0();
  float t[4]; for(int k=0;k<4;k++) t[k]=s0[k][0]+s0[k][1];
  SB0();
  for(int k=0;k<4;k++) s0[k]=xh(t[k]);
  SB0();
  for(int k=0;k<4;k++) t[k]=s0[k][0]+s0[k][1];
  SB0();
  for(int k=0;k<4;k++) t[k]=__builtin_amdgcn_rcpf(t[k]);
  SB0(); hook(3); SB0();
  for(int a=0;a<2;a++) for(int i=0;i<5;i++){ ep[a][i]*=t[a]; en[a][i]*=t[2+a]; }
  SB0();
  for(int a=0;a<2;a++) for(int i=0;i<5;i++){ o[a][i]=pk2(ep[a][i][0],ep[a][i][1]); o[a][5+i]=pk2(en[a][i][0],en[a][i][1]); }
  SB0();
}
DI v4u pack8r(const f32x16& a, int r0){ return v4u{pk2(a[r0],a[r0+1]),pk2(a[r0+2],a[r0+3]),pk2(a[r0+4],a[r0+5]),pk2(a[r0+6],a[r0+7])}; }

struct Args { const u16 *x,*wqkv,*wphi,*wo,*wg,*wu,*wd; u16* out; char* ws; };
constexpr unsigned M_W=0, M_XS=65536, M_RT=114688, M_PHI=79872, M_VI=M_PHI+128*168, M_WO=0;
constexpr unsigned M_CSX=L_CSX, M_KVI=L_KVI, M_WP=L_KVI+4224;
template<int OFF> DI void dmaN(v4u rs, unsigned voff, unsigned soff){
  asm volatile("buffer_load_dwordx4 %0, %1, %2 offen offset:%3 lds" :: "v"(voff), "s"(rs), "s"(soff), "n"(OFF) : "memory");
}
DI void dma0nt(v4u rs, unsigned lds, unsigned voff, unsigned soff){
  asm volatile("s_mov_b32 m0, %0\n\ts_nop 0\n\tbuffer_load_dwordx4 %1, %2, %3 offen nt lds" :: "s"(lds), "v"(voff), "s"(rs), "s"(soff) : "memory");
}
template<int OFF> DI void dmaNnt(v4u rs, unsigned voff, unsigned soff){
  asm volatile("buffer_load_dwordx4 %0, %1, %2 offen offset:%3 nt lds" :: "v"(voff), "s"(rs), "s"(soff), "n"(OFF) : "memory");
}
DI void st16a(v4u d, v4u rs, unsigned voff, unsigned soff){
  asm volatile("buffer_store_dwordx4 %0, %1, %2, %3 offen sc1" :: "a"(d), "v"(voff), "s"(rs), "s"(soff) : "memory");
}
DI unsigned sel(bool c, unsigned a, unsigned b){ return c? a:b; }
DI void touch(unsigned& d, v4u rs, unsigned voff, unsigned soff){
  asm volatile("buffer_load_dword %0, %1, %2, %3 offen" : "=v"(d) : "v"(voff), "s"(rs), "s"(soff) : "memory");
}

__global__ __launch_bounds__(256,1) void blk_k(Args ar){
  extern __shared__ __attribute__((aligned(16))) char smem[];
  char* ws=ar.ws;
  Ctx c;
  c.ws=ws; c.b=blockIdx.x; c.lane=threadIdx.x&63; c.w=__builtin_amdgcn_readfirstlane(threadIdx.x>>6);
  c.pi=((c.b&7)>>1)*4+((c.b>>3)&3); c.pj=(c.b&1)*8+(c.b>>5);
  const int lane=c.lane, w=c.w, pi=c.pi, pj=c.pj, jq=pj^1, tid=threadIdx.x;
  stamp(ws,c.b,0);
  unsigned dummy;
  { u64 pc; unsigned len; asm volatile("s_getpc_b64 %0\n.Lhg_code_begin:\n\ts_mov_b32 %1, .Lhg_code_end-.Lhg_code_begin" : "=s"(pc), "=s"(len));
    touch(dummy, mkrs((const void*)pc,len), sel(lane<4,lane*128,OOB), ((c.b>>3)*16+w*4)*128); }
  const int lid=pi*16+pj; const bool job = lid<216;
  const int jp=(lid*152)>>12, jl=lid-jp*27, ju=lid*2+(w>>1), js=ju>>3;
  const v4u rx=mkrs(ar.x,S*D*2), rw=mkrs(ar.wqkv,D*1920*2);
  int kph = ROT? (3*pi+pj)%10 : 0;
  const unsigned xs=M_XS+w*12288, xrow=(pi*128+w*32)*1280, wrow=w*16*3840;
  const int g4=lane>>4, tk=lane&15;
  unsigned xv[4]; for(int e=0;e<4;e++) xv[e]=(lane>>3)*1280+(((lane&7)^(g4+4*(e&1))))*16+e*(10240-1024);
  unsigned wv[4];
  { const unsigned ch=(tk-4*g4)&15; unsigned cb=sel(ch<5,1280+pj*80,2480+pj*80); cb=sel(ch<10,cb,jq*80-160); cb=sel(ch<15,cb,OOB);
    wv[0]=g4*3840+ch*16+cb; for(int p=1;p<4;p++) wv[p]=wv[0]+p*(15360-1024); }
  unsigned xso=xrow+kph*128, wso=wrow+kph*(64*3840);
  auto ldp=[&](int kc,auto P) AI { constexpr int p=P;
    if constexpr(p==0) dma0(rx, xs+(kc%3)*4096, xv[0], xso); else if constexpr(p<4) dmaN<p*1024>(rx,xv[p&3],xso);
    else if constexpr(p==4) dma0(rw, M_W+(kc%4)*16384+w*4096, wv[0], wso); else dmaN<(p-4)*1024>(rw,wv[(p-4)&3],wso);
    if constexpr(p==7){ kph = kph==9? 0:kph+1; xso=xrow+kph*128; wso=wrow+kph*(64*3840); } };
  UNROLL(8,I,ldp(0,IC<I>{}));
  SB0();
  stamp(ws,c.b,29);
  c.rs=mkrs(ws,WS_END); c.voff=lane*16; c.nretry=0;
  const int tok=lane&31, hh=lane>>5;
  const int cc=pj&7, rh=pj>>3, th=w>>1, kh=w&1, t16=pi*8+rh*4+w;
  const int blk=g4&1, q4=tk>>2, p4=lane&3, qd=blk*4+p4;
  v4u PZ={POISON,POISON,POISON,POISON}; pinV(PZ);
  const unsigned osb=WS_O+(pi*4+w)*40960+jq*1280, hsb=WS_H+(t16>>1)*40960+cc*5120+(t16&1)*256, ssb=WS_ST+pi*8192+cc*1024+(rh*64+w*16)*8;
  unsigned ov=blk*20480+tk*16, ov1=ov+hh*256, ov2=sel(hh,OOB,ov+512), kvo=sel(tid<220,tid*16,OOB);
  unsigned hv1=tk*16+g4*512, hv2=sel(blk,OOB,tk*16+hh*512), sv=sel(lane<16,tk*8,OOB);
  unsigned pfo=sel(lane<16,tk*16+jq,pi*16+tk)*128, xo_=tk*1280+g4*16, xo2=xo_-g4*8;
  c.flagv=sel(lane==0,lid*128,OOB); PIN(c.flagv);
  PIN(ov1); PIN(ov2); PIN(kvo); PIN(hv1); PIN(hv2); PIN(sv); PIN(pfo); PIN(xo_); PIN(xo2);
  float sm[2]={0.f,0.f}, sq[2]={0.f,0.f};
  unsigned C11=0x3f803f80u; PIN(C11);
  unsigned xr[4]; { unsigned b0=xs+tok*128, x0=(hh^((tok>>1)&7))*16; for(int s=0;s<4;s++){ xr[s]=b0+(x0^(32*s)); PIN(xr[s]); } }
  v4u ONE; { unsigned o1=sel(tok==0,0x3f803f80u,0u); ONE=v4u{o1,o1,o1,o1}; pinV(ONE); }
  unsigned ab[4], aw[4];
  { const unsigned rb=M_W+256*(8*hh+q4), rq=8*q4; const bool lo=qd<5;
    ab[0]=rb+8*((rq+qd+sel(lo,0,8))&31); ab[1]=rb+8*((rq+qd+5)&31); ab[2]=rb+8*((rq+qd+sel(lo,20,12))&31); ab[3]=rb+8*((rq+sel(lo,25+qd,16))&31);
    for(int t=0;t<4;t++){ aw[t]=ab[t]+w*4096; PIN(ab[t]); PIN(aw[t]); } }
  const unsigned qd4=8*min(qd,4);
  unsigned pwb=M_WP+q4*40+qd4, pwa=pwb+hh*160; PIN(pwa); PIN(pwb);
  unsigned csw=sel(hh,147456u,M_CSX)+lane*16+w*4, csr=M_CSX+tok*16; PIN(csw); PIN(csr);
  unsigned prow=M_PHI+(w*32+tok)*168+hh*40, vrow=M_VI+(w*32+tok)*104; PIN(prow);
  unsigned vwa[4], vwb[2];
  vwa[0]=vrow+40-16*hh; vwa[1]=vrow+88-88*hh; vwa[2]=vrow+40+24*hh; vwa[3]=vrow+88-32*hh;
  vwb[0]=vrow+32+16*hh; vwb[1]=vrow+8+8*hh;
  for(int t=0;t<4;t++) PIN(vwa[t]); for(int t=0;t<2;t++) PIN(vwb[t]);
  unsigned pa=M_PHI+(8*hh+q4)*168+w*40+qd4, va=M_VI+(8*hh+q4)*104+(w>>1)*48+8*min(qd,5), kvs=sel(tok<22,tok*8+hh*176,OOB), kvs2=sel(hh,OOB,kvs);
  PIN(pa); PIN(va); PIN(kvs); PIN(kvs2);
  *(LAS v2u*)(sel(tid<88,M_KVI+tid*48+40,150528u))=v2u{0,0};
  v4u ro=mkrs(ar.wo,D*D*2), rg=mkrs(w&1? ar.wu:ar.wg,job? D*F*2:0), rd=mkrs(ar.wd,job? D*F*2:0); PINS(ro); PINS(rg); PINS(rd);
  unsigned s3o=(jp*80+(w>>1)*40)*3456+jl*128, s4o=(js*32+(w&1)*16)*1280+(ju&7)*160, rtv=(lane>>3)*3456+(lane&7)*16, rdv[3];
  for(int p=0;p<3;p++){ int idx=p*64+lane, n=(idx*205)>>11; rdv[p]=n*(1280-160)+lane*16; if(p==2) rdv[p]=sel(lane<32,rdv[p],OOB); PIN(rdv[p]); }
  PINS(s3o); PINS(s4o); PIN(rtv);
  auto rtp=[&](auto P) AI { constexpr int p=P;
    if constexpr(p==0) dma0nt(rg,M_RT+(w&1)*10240+(w>>1)*5120,rtv,s3o);
    else if constexpr(p<4) dmaNnt<p*1024>(rg,rtv,s3o+p*(8*3456-1024));
    else if constexpr(p==4) dma0nt(rg,M_RT+(w&1)*10240+(w>>1)*5120+4096,rtv,s3o+32*3456);
    else if constexpr(p==5) dma0nt(rd,M_RT+20480+w*3072,rdv[0],s4o); else dmaNnt<(p-5)*1024>(rd,rdv[(p-5)%3],s4o); };
  unsigned wov=sel(lane<50,lane*128,OOB); PIN(wov);
  unsigned ovv[5];
  for(int p=0;p<5;p++){ int idx=p*64+lane, n=(idx*205)>>11; int row=(n&~12)|((n&4)<<1)|((n&8)>>1); ovv[p]=row*1280-n*160+lane*16+(p&4)*1024; PIN(ovv[p]); }
  const int orot=OROT? (5*cc)>>1 : 0;
  auto wop=[&](auto N) AI { constexpr int n=N, p=n%5; int s=(n/5)*4+w, sp=s+orot; sp= sp>=20? sp-20:sp; unsigned so=sp*32*1280+cc*160;
    if constexpr(p%4==0) dma0(ro,M_WO+s*5120+p*1024,ovv[p],so); else dmaN<p*1024>(ro,ovv[p],so); };
  SB0();
  stamp(ws,c.b,27);
  v2u ep; ld8(ep,c.rs,0u,WS_EPOCH);
  if(lane<50) dma0(mkrs(ar.wphi,NH*400*2), M_WP+w*800, c.voff, (pj>>1)*3200+w*800);
  UNROLL(8,I,ldp(1,IC<I>{}));
  stamp(ws,c.b,28);
  f32x16 acc[4], acs[4]; v4u xt[2][4]; v4u PW_[4][2]; v2u pf; v4u xr0,xr1; v2u xr2;
  float rstd1, mean1; v4u MB1; f32x4 cv[4]; float cs[4]; v4u cf[4];
  vmwait<10>(); barrier();
  stamp(ws,c.b,1);
  for(int s=0;s<4;s++) xt[0][s]=*(const LAS v4u*)(xr[s]);
  pipe<200,4>(
    [&](auto i) AI { constexpr int I=i, kc=I/20, r=I%20, t=r&3, o0=(kc%4)*16384; unsigned a=(r<4? aw[t]+o0 : ab[t]+o0+((r-4)/4)*4096); return trfrag(a,a+1024); },
    [&](auto i,v4u A) AI { constexpr int I=i, kc=I/20, r=I%20, t=r&3;
      if constexpr(r<4){ if constexpr(kc==0) mfma32A_0(acs[t],ONE,A); else mfma32A(acs[t],ONE,A); }
      else { if constexpr(kc==0 && r<8) mfma32vv_0(acc[t],A,xt[0][0]); else mfma32vv(acc[t],A,xt[kc&1][(r-4)/4]); } },
    [&](auto i) AI { constexpr int I=i, kc=I/20, r=I%20;
      if constexpr(r%2==0 && r<16 && kc<8) ldp(kc+2,IC<(r/2)&7>{});
      if constexpr(kc==8){
        if constexpr(r==0) asm volatile("buffer_load_dwordx4 %0, %1, %2, %3 offen" : "=v"(xr0) : "v"(xo_), "s"(rx), "s"(t16*20480+cc*160) : "memory");
        if constexpr(r==1) asm volatile("buffer_load_dwordx4 %0, %1, %2, %3 offen offset:64" : "=v"(xr1) : "v"(xo_), "s"(rx), "s"(t16*20480+cc*160) : "memory");
        if constexpr(r==2) asm volatile("buffer_load_dwordx2 %0, %1, %2, %3 offen offset:128" : "=v"(xr2) : "v"(xo2), "s"(rx), "s"(t16*20480+cc*160) : "memory");
        if constexpr(r==5) st16(PZ,c.rs,kvo,WS_KVP+(pj*16+pi)*KVP_B);
        if constexpr(r==7) st16(PZ,c.rs,ov1,osb);
        if constexpr(r==9) st16(PZ,c.rs,ov1,osb+768);
        if constexpr(r==11) st16(PZ,c.rs,ov2,osb);
        if constexpr(r==13) st16(PZ,c.rs,hv1,hsb);
        if constexpr(r==15) st16(PZ,c.rs,hv1,hsb+2048);
        if constexpr(r==17) st16(PZ,c.rs,hv2,hsb+4096);
        if constexpr(r==19) st8(v2u{PZ.x,PZ.y},c.rs,sv,ssb); }
      if constexpr(kc==9 && r%2==1 && r<17) rtp(IC<(r/2)&7>{});
      if constexpr(kc==9 && r==19) touch(dummy,ro,wov,((c.b>>3)*200+w*50)*128);
      if constexpr(r==3){ for(int s=0;s<4;s++){ v4u v=xt[kc&1][s]; sm[0]=dot2(v.x,C11,sm[0]); sq[0]=dot2(v.x,v.x,sq[0]); sm[1]=dot2(v.y,C11,sm[1]); sq[1]=dot2(v.y,v.y,sq[1]);
          sm[0]=dot2(v.z,C11,sm[0]); sq[0]=dot2(v.z,v.z,sq[0]); sm[1]=dot2(v.w,C11,sm[1]); sq[1]=dot2(v.w,v.w,sq[1]); } PIN(sm[0]); PIN(sm[1]); PIN(sq[0]); PIN(sq[1]); }
      if constexpr(r==16 && kc<9){
        if constexpr(kc==8) vmwait<9>(); else vmwait<8>();
        barrier();
        if constexpr(KVST && (kc<4 || kc>=6)) stamp(ws,c.b,8+(kc<4? kc:kc-2));
        if constexpr(kc==1){ asm volatile("" : "+v"(ep),"+v"(dummy));
          { u64 t=(((u64)ep.y<<32)|ep.x)+0x9E3779B97F4A7C15ull; c.tag=v2u{(unsigned)t,(unsigned)(t>>32)}; asm volatile("" : "+v"(c.tag)); } } }
      if constexpr(r>=16 && kc<9) xt[(kc+1)&1][r&3]=*(const LAS v4u*)(xr[r&3]+((kc+1)%3)*4096);
      if constexpr(kc>=4 && kc<8 && (r==5||r==7||r==11)){ constexpr int a=kc-4;
        unsigned ho=((a<2? (pj&1):(jq&1))*2+(a&1))*800;
        if constexpr(r==5){ v2u v=trd(pwa+ho), u=trd(pwa+ho+320); PW_[a][0]=v4u{v.x,v.y,u.x,u.y}; }
        if constexpr(r==7){ v2u v=trd(pwb+ho+640); PW_[a][1]=v4u{v.x,v.y,0u,0u}; }
        if constexpr(r==11){ PW_[a][1].x= hh? 0u:PW_[a][1].x; PW_[a][1].y= hh? 0u:PW_[a][1].y; pinV(PW_[a][0]); pinV(PW_[a][1]); } }
      if constexpr(kc==9){
        if constexpr(r==4){ asm volatile("" : "+a"(acs[0]),"+a"(acs[1]),"+a"(acs[2]),"+a"(acs[3])); for(int t=0;t<4;t++) *(LAS float*)(csw+t*512)=acs[t][0]; }
        if constexpr(r==5){ sm[0]+=sm[1]; sq[0]+=sq[1]; PIN(sm[0]); PIN(sq[0]); }
        if constexpr(r==6){ sm[0]=xh_add(sm[0]); sq[0]=xh_add(sq[0]); PIN(sm[0]); PIN(sq[0]); }
        if constexpr(r==7){ mean1=sm[0]*(-1.f/640.f); PIN(mean1); sq[0]=fmaxf(sq[0]*(1.f/640.f)-mean1*mean1,0.f)+1e-5f; PIN(sq[0]); }
        if constexpr(r==8){ lgk0(); barrier(); for(int t=0;t<4;t++) cv[t]=*(const LAS f32x4*)(csr+t*512); }
        if constexpr(r==9){ rstd1=__builtin_amdgcn_rsqf(sq[0]); PIN(rstd1); unsigned hi=pk2(mean1,0.f); MB1.y=hi; PIN(MB1.y); }
        if constexpr(r==10){ MB1.x=pk2(mean1,mean1-blo(MB1.y)); PIN(MB1.x); }
        if constexpr(r==11){ MB1= hh? v4u{0,0,0,0} : v4u{MB1.x,MB1.y,0,0}; pinV(MB1); }
        if constexpr(r==12){ for(int t=0;t<4;t++){ cs[t]=cv[t][0]+cv[t][1]; PIN(cs[t]); } }
        if constexpr(r==13){ for(int t=0;t<4;t++){ cs[t]+=cv[t][2]; PIN(cs[t]); } }
        if constexpr(r==14){ for(int t=0;t<4;t++){ cs[t]+=cv[t][3]; PIN(cs[t]); } }
        if constexpr(r==15){ for(int t=0;t<4;t++){ cf[t].x=pk2(cs[t],cs[t]); PIN(cf[t].x); } }
        if constexpr(r==16){ for(int t=0;t<4;t++){ cf[t].y=cf[t].x<<16; PIN(cf[t].y); } }
        if constexpr(r==17){ for(int t=0;t<4;t++){ cs[t]-=__uint_as_float(cf[t].y); PIN(cs[t]); } }
        if constexpr(r==18){ for(int t=0;t<4;t++){ cf[t].y=pk2(cs[t],0.f); cf[t].z=cf[t].y; cf[t].w=cf[t].y; pinV(cf[t]); } }
      }
    });
  for(int t=0;t<4;t++) mfma32vv(acc[t],cf[t],MB1);
  stamp(ws,c.b,2);
  v4u q0[2],q1[2];
  { const float vs=hh? rstd1:1.f;
    f32x16 pz[2]; v4u k0[2],k1[4];
    asm volatile("s_nop 7" : "+v"(acc[0]),"+v"(acc[1]));
    f32x2 m_[4][4];
    for(int a=0;a<2;a++) k0[a]=pack8r(acc[a],0);
    SB0();
    for(int a=0;a<2;a++) for(int e=0;e<4;e++) m_[a][e]=f32x2{acc[a][8+2*e],acc[a][9+2*e]}*(e<2? vs:rstd1);
    SB0();
    for(int a=0;a<2;a++){ k1[a]=v4u{pk2(m_[a][0][0],m_[a][0][1]),pk2(m_[a][1][0],m_[a][1][1]),pk2(m_[a][2][0],m_[a][2][1]),pk2(m_[a][3][0],m_[a][3][1])}; }
    asm volatile("" : "+v"(acc[2]),"+v"(acc[3]));
    for(int a=2;a<4;a++) for(int e=0;e<(a==2?4:2);e++) m_[a][e]=f32x2{acc[a][8+2*e],acc[a][9+2*e]}*(e<2? vs:rstd1);
    for(int a=0;a<2;a++){ pinV(k0[a]); pinV(k1[a]); }
    SB0();
    for(int a=0;a<2;a++){ mfma32vv_0(pz[a],PW_[a][0],k0[a]); mfma32vv(pz[a],PW_[a][1],k1[a]); }
    SB0();
    k1[2]=v4u{pk2(m_[2][0][0],m_[2][0][1]),pk2(m_[2][1][0],m_[2][1][1]),pk2(m_[2][2][0],m_[2][2][1]),pk2(m_[2][3][0],m_[2][3][1])};
    k1[3]=v4u{pk2(m_[3][0][0],m_[3][0][1]),pk2(m_[3][1][0],m_[3][1][1]),0,0};
    for(int a=0;a<2;a++){ q0[a]=pack8r(acc[2+a],0); q1[a]=k1[2+a]; pinV(q0[a]); pinV(q1[a]); }
    for(int t=0;t<4;t++) *(LAS v2u*)(vwa[t]) = v2u{sel(hh,k1[t].x,0x00003f80u),sel(hh,k1[t].y,0u)};
    *(LAS v2u*)(vwb[0]) = v2u{k1[0].z,k1[0].w}; *(LAS v2u*)(vwb[1]) = v2u{k1[1].z,k1[1].w}; *(LAS v2u*)(vwb[1]+64) = v2u{k1[2].z,k1[2].w};
    SB0(); asm volatile("" : "+v"(pz[0]),"+v"(pz[1]));
    unsigned ph[2][10]; hedgehog2(pz[0],pz[1],hh,rstd1*LOG2E,ph,[&](int k) AI {});
    for(int a=0;a<2;a++) for(int i=0;i<5;i++) *(LAS v2u*)(prow+a*80+i*8)=v2u{ph[a][2*i],ph[a][2*i+1]};
  }
  vmwait<9>(); lgk0(); barrier();
  asm volatile("" : "+v"(xr0),"+v"(xr1),"+v"(xr2));
  if(w==1) st8(c.tag,c.rs,c.flagv,WS_FLAG+FK_A*256*128);
  stamp(ws,c.b,3);
  f32x16 pq[2];
  { v4u fa[8],fb[8];
    #pragma unroll
    for(int s=0;s<8;s++){ fa[s]=trfrag(pa+s*2688,pa+s*2688+672); fb[s]=trfrag(va+s*1664,va+s*1664+416);
      if(s==1){ SB0(); for(int a=0;a<2;a++){ mfma32vv_0(pq[a],PW_[2+a][0],q0[a]); mfma32vv(pq[a],PW_[2+a][1],q1[a]); } SB0(); } }
    SB0();
    f32x16 kv; mfma32vv_0(kv,fa[0],fb[0]);
    #pragma unroll
    for(int s=1;s<8;s++) mfma32vv(kv,fa[s],fb[s]);
    mdone(kv);
    const unsigned so=WS_KVP+(pj*16+pi)*KVP_B+w*880;
    st8(v2u{pk2(kv[0],kv[1]),pk2(kv[2],kv[3])},c.rs,kvs,so);
    st8(v2u{pk2(kv[4],kv[5]),pk2(kv[6],kv[7])},c.rs,kvs,so+352);
    st8(v2u{pk2(kv[8],kv[9]),pk2(kv[10],kv[11])},c.rs,kvs2,so+704);
  }
  stamp(ws,c.b,4);
  unsigned phq[2][10];
  { unsigned r3=M_RT+blk*10240+(8*hh+q4)*128+8*(((p4&1)<<1)|(p4>>1))+w*32, csv=sel(hh,OOB,tok*16);
    unsigned r4=M_RT+20480+(w>>1)*6144+hh*3072+(8*blk+q4)*160, r4a=r4+64*(w&1)+16*p4, r4b=r4+128+8*p4;
    const int tau=jl*4+w, j2= tau<96? (tau*43)>>8 : tau-96, i2= tau<96? tau-j2*6 : 6, js3=(js*43)>>7;
    const int sl0=(jp-(XROT? (j2&7):0))&7;
    const unsigned d3= job? WS_WT3+j2*286720+(i2<PW? PW*sl0*5+i2 : 40*i2+sl0*5)*1024 : WS_END, d3s= i2<PW? PW*1024:1024;
    const unsigned d4= job? WS_WT4+(ju&7)*286720+(js<48? 16*(js-3*js3)+((js3-(AROT? 2*(ju&7):0))&15) : js)*5120 : WS_END, dcs=job? WS_CS3+j2*8192+i2*1024+(jp>>2)*512+(jp&3)*4:WS_END;
    vmwait<4>(); barrier();
    stamp(ws,c.b,30);
    v4u rf[8];
    for(int s=0;s<5;s++) rf[s]=trfrag(r3+s*2048,r3+s*2048+512);
    rf[5]=trfrag(r4a,r4a+640); rf[6]=trfrag(r4a+8,r4a+648); rf[7]=trfrag(r4b,r4b+640);
    ld8(pf,c.rs,pfo,WS_FLAG+FK_A*256*128);
    UNROLL(5,I,wop(IC<I>{}));
    SB0();
    f32x16 rcs;
    #pragma unroll
    for(int s=0;s<5;s++){ if(s==0) mfma32vv_0(rcs,ONE,rf[0]); else mfma32vv(rcs,ONE,rf[s]); if(s<2) st16(rf[s],c.rs,c.voff,d3+s*d3s); }
    stamp(ws,c.b,5);
    asm volatile("" : "+v"(pq[0]),"+v"(pq[1]));
    hedgehog2(pq[0],pq[1],hh,rstd1*LOG2E,phq,[&](int k) AI {
      if(k==0){ st16(rf[2],c.rs,c.voff,d3+2*d3s); st16(rf[3],c.rs,c.voff,d3+3*d3s); wop(IC<5>{}); }
      if(k==1){ st16(rf[4],c.rs,c.voff,d3+4*d3s); st16(rf[5],c.rs,c.voff,d4+(w&1)*2048); wop(IC<6>{}); }
      if(k==2){ st16(rf[6],c.rs,c.voff,d4+(w&1)*2048+1024); st16(rf[7],c.rs,c.voff,(w&1)? d4+4096:WS_END); wop(IC<7>{}); }
      if(k==3){ asm volatile("" : "+v"(rcs)); float ct=rcs[0]; unsigned hi=pk2(ct,0.f); st4(pk2(ct,ct-blo(hi)),c.rs,csv,dcs); wop(IC<8>{}); wop(IC<9>{}); } });
  }
  v4u pb[2][3];
  for(int a=0;a<2;a++){ pb[a][0]=v4u{phq[a][0],phq[a][1],phq[a][2],phq[a][3]}; pb[a][1]=v4u{phq[a][4],phq[a][5],phq[a][6],phq[a][7]}; pb[a][2]=v4u{phq[a][8],phq[a][9],0,0}; for(int s=0;s<3;s++) pinV(pb[a][s]); }
  stamp(ws,c.b,6);
  unsigned kra[2], wb[2][3]; v2u wf0,wf1;
  unsigned wfo0, wfo1;
  { const int l3=lane>>3; unsigned ln=sel(l3==0,(6*pj)>>2,(6*pj+5)>>2); ln=sel(l3>=2 && pj<12,24+(pj>>2),ln); wfo0=((lane&7)*27+ln)*128; wfo1=(4*min(lane,53)+(cc>>1))*128; }
  vmwait<19>(); asm volatile("" : "+v"(pf),"+v"(dummy));
  for(int sp=0;sp<SPIN_CAP;++sp){
    if(!__any(((pf.x^c.tag.x)|(pf.y^c.tag.y))!=0)) break;
    ld8(pf,c.rs,pfo,WS_FLAG+FK_A*256*128); vmwait<0>(); asm volatile("" : "+v"(pf)); }
  stamp(ws,c.b,25);
  { v4u kp[16];
    UNROLL(16,I,ldV<0>(kp[I],c.rs,kvo,WS_KVP+(jq*16+I)*KVP_B));
    unsigned C01=0x00003f80u, C10=0x3f800000u; PIN(C01); PIN(C10);
    unsigned kia; { int wq=(tid*1192)>>16, r=tid-wq*55, qu=(r*187)>>11, dp=r-qu*11; kia=sel(tid<220,M_KVI+(((wq>>1)*22+2*dp)*2+(wq&1))*48+qu*8,150528u); PIN(kia); }
    { const int h2=(tok>>2)&1, q=(tok&3)+4*(tok>>3);
      for(int a=0;a<2;a++){ unsigned d=sel(q==12,20,21); d=sel(q<12 && !h2,a==0? 8+q:q-8,d); d=sel(q<8,4*a+8*h2+q,d); kra[a]=M_KVI+a*2112+d*96+hh*48; PIN(kra[a]); } }
    { const unsigned rb=M_WO+160*(16*hh+4*blk+q4);
      wb[0][0]=rb+16*p4+8*(p4>>1); wb[0][1]=wb[0][0]^8; wb[0][2]=rb+128+8*p4;
      for(int t=0;t<3;t++){ wb[1][t]=wb[0][t]+61440; PIN(wb[0][t]); PIN(wb[1][t]); } }
    UNROLL(5,I,wop(IC<10+I>{}));
    vmwait<5>(); UNROLL(16,I,pinV(kp[I]));
    for(int sp=0;sp<SPIN_CAP;++sp){
      unsigned m0=0,m1=0; for(int i=0;i<8;i++){ m0=max(m0,max(kp[i].x,kp[i].z)); m1=max(m1,max(kp[8+i].x,kp[8+i].z)); }
      if(!__any(max(m0,m1)==POISON)) break;
      c.nretry+=1<<24;
      UNROLL(16,I,ldV<0>(kp[I],c.rs,kvo,WS_KVP+(jq*16+I)*KVP_B)); vmwait<0>(); UNROLL(16,I,pinV(kp[I]));
    }
    stamp(ws,c.b,23);
    UNROLL(5,I,wop(IC<15+I>{}));
    float s8[8]={};
    for(int i=0;i<16;i++){
      s8[0]=dot2(kp[i].x,C01,s8[0]); s8[1]=dot2(kp[i].x,C10,s8[1]); s8[2]=dot2(kp[i].y,C01,s8[2]); s8[3]=dot2(kp[i].y,C10,s8[3]);
      s8[4]=dot2(kp[i].z,C01,s8[4]); s8[5]=dot2(kp[i].z,C10,s8[5]); s8[6]=dot2(kp[i].w,C01,s8[6]); s8[7]=dot2(kp[i].w,C10,s8[7]); SB0(); }
    *(LAS v2u*)kia=v2u{pk2(s8[0],s8[1]),pk2(s8[2],s8[3])}; *(LAS v2u*)(kia+96)=v2u{pk2(s8[4],s8[5]),pk2(s8[6],s8[7])};
    vmwait<5>(); lgk0(); barrier();
    if(w==2) st8(c.tag,c.rs,c.flagv,WS_FLAG+FK_W*256*128);
  }
  stamp(ws,c.b,24);
  { v4u ka[2][3];
    for(int a=0;a<2;a++) for(int s=0;s<3;s++) ka[a][s]=*(const LAS v4u*)(kra[a]+16*s);
    SB0();
    f32x16 o[2];
    for(int a=0;a<2;a++){ mfma32vv_0(o[a],ka[a][0],pb[a][0]); mfma32vv(o[a],ka[a][1],pb[a][1]); mfma32vv(o[a],ka[a][2],pb[a][2]); }
    mdone(o[1]); asm volatile("" : "+v"(o[0]));
    float r[2]; for(int a=0;a<2;a++) r[a]=__builtin_amdgcn_rcpf(o[a][12]+1e-6f);
    SB0();
    f32x2 y[2][6]; for(int a=0;a<2;a++) for(int e=0;e<6;e++) y[a][e]=f32x2{o[a][2*e],o[a][2*e+1]}*r[a];
    SB0();
    unsigned u_[2][6]; for(int a=0;a<2;a++) for(int e=0;e<6;e++) u_[a][e]=pk2(y[a][e][0],y[a][e][1]);
    st16(v4u{u_[0][0],u_[0][1],u_[0][2],u_[0][3]},c.rs,ov1,osb); st16(v4u{u_[1][0],u_[1][1],u_[1][2],u_[1][3]},c.rs,ov1,osb+768); st16(v4u{u_[0][4],u_[0][5],u_[1][4],u_[1][5]},c.rs,ov2,osb);
  }
  stamp(ws,c.b,7);
  UNROLL(5,I,wop(IC<20+I>{}));
  c.src3 = WS_WT3 + pj*286720 + w*5120;
  c.src4 = WS_WT4 + cc*286720 + w*5120;
  c.wlo = WS_WT4 + cc*286720 + 240*1024 + kh*5120;
  v4u hr0,hr1; v2u hr2;
  { v4u WA[12][5];
    unsigned pr, prv=(lane>>2)*1280+512+(lane&3)*64;
    v4u WB[4][5]; v4u O[20]; const unsigned oo=WS_O+t16*20480;
    auto oload=[&](auto i) AI { constexpr int s=i; int sp=s+orot; sp= sp>=20? sp-20:sp; ldV<0>(O[s],c.rs,c.voff,oo+sp*1024); };
    auto pre=[&](auto i) AI { constexpr int s=i;
      if constexpr(s==12){ vmwait<8+(OL0<12? 1+3*(12-OL0):0)>(); barrier(); }
      #pragma unroll
      for(int u=0;u<5;u++){
        if constexpr(s<12){ unsigned a=wb[0][u<4? u&1:2]+s*5120+(u<4? 64*(u>>1):0); WA[s][u]=trfrag(a,a+1280); }
        else if constexpr(s<16){ unsigned a=wb[1][u<4? u&1:2]+(s-12)*5120+(u<4? 64*(u>>1):0); WB[s-12][u]=trfrag(a,a+1280); } }
      SB0();
      if constexpr(s==OL0) asm volatile("buffer_load_dword %0, %1, %2, %3 offen sc1" : "=v"(pr) : "v"(prv), "s"(c.rs), "s"(oo) : "memory");
      if constexpr(s>=OL0 && s<OL0+4) UNROLL(3,J,oload(IC<(s-OL0)*3+J>{}));
      #pragma unroll
      for(int u=0;u<5;u++){ if constexpr(s>=1 && s<13) pinA(WA[s-1][u]); else if constexpr(s>=13) pinV(WB[s-13][u]); }
      SB0(); };
    UNROLL(17,I,pre(IC<I>{}));
    stamp(ws,c.b,16);
    vmwait<12>();
    barrier();
    stamp(ws,c.b,17);
    f32x4 a2[5];
    v4u WC[4][5];
    unsigned omax;
    auto p2step=[&](auto i) AI { constexpr int I=i, s=I/5, u=I%5;
      if constexpr(u==0 && s%2==0) vmwait<(s<=10? 10:20-s)>();
      if constexpr(s==0) mfma16a_0(a2[u],WA[0][u],O[0]);
      else if constexpr(s<12) mfma16a(a2[u],WA[s<12?s:0][u],O[s]);
      else if constexpr(s<16) mfma16(a2[u],WB[(s-12)&3][u],O[s]);
      else mfma16(a2[u],WC[(s-16)&3][u],O[s]);
      SB0();
      if constexpr(u==1){ if constexpr(s<8) oload(IC<(12+s)%20>{}); else if constexpr(s==8) ld8(wf0,c.rs,wfo0,WS_FLAG+FK_W*256*128); else if constexpr(s==9) ld8(wf1,c.rs,wfo1,WS_FLAG+FK_W*256*128); }
      if constexpr(u==3 && s%2==0) asm volatile("v_max3_u32 %0, %0, %1, %2" : "+v"(omax) : "v"(O[s].x), "v"(O[s+1].x));
      if constexpr(I>=20 && I<60 && I%2==0){ constexpr int n=(I-20)/2, s2=n/5, u2=n%5; unsigned a=wb[1][u2<4? u2&1:2]+(4+s2)*5120+(u2<4? 64*(u2>>1):0); WC[s2][u2]=trfrag(a,a+1280); }
      SB0(); };
    auto again=[&]() AI {
      c.nretry+=1<<28; vmwait<0>();
      for(int s2=0;s2<SPIN_CAP;++s2){
        asm volatile("buffer_load_dword %0, %1, %2, %3 offen sc1\n\ts_waitcnt vmcnt(0)" : "=v"(pr) : "v"(prv), "s"(c.rs), "s"(oo) : "memory");
        if(!__any(pr==POISON)) break; }
      UNROLL(12,J,oload(IC<J>{})); };
    PIN(pr);
    if(__builtin_expect(__any(pr==POISON),0)) again();
    for(int sp=0;;++sp){
      omax=0;
      UNROLL(100,I,p2step(IC<I>{}));
      if(!__any(omax==POISON) || sp>=REDO_CAP) break;
      again(); vmwait<0>(); UNROLL(12,J,pinV(O[J]));
    }
    f32x2 xf[10]; { unsigned xw[10]={xr0.x,xr0.y,xr0.z,xr0.w,xr1.x,xr1.y,xr1.z,xr1.w,xr2.x,xr2.y}; for(int i=0;i<10;i++) xf[i]=f32x2{blo(xw[i]),bhi(xw[i])}; }
    SB0();
    asm volatile("" : "+v"(a2[0]),"+v"(a2[1]),"+v"(a2[2]),"+v"(a2[3]),"+v"(a2[4]));
    stamp(ws,c.b,26);
    const bool sw=g4>=2;
    float hv_[20];
    for(int r=0;r<4;r++){ hv_[r]= sw? a2[1][r]:a2[0][r]; hv_[4+r]= sw? a2[0][r]:a2[1][r]; hv_[8+r]= sw? a2[3][r]:a2[2][r]; hv_[12+r]= sw? a2[2][r]:a2[3][r]; hv_[16+r]=a2[4][r]; }
    SB0();
    for(int i=0;i<10;i++) xf[i]+=f32x2{hv_[2*i],hv_[2*i+1]};
    SB0();
    unsigned hw[10]; for(int i=0;i<10;i++) hw[i]=pk2(xf[i][0],xf[i][1]);
    hr0=v4u{hw[0],hw[1],hw[2],hw[3]}; hr1=v4u{hw[4],hw[5],hw[6],hw[7]}; hr2=v2u{hw[8],hw[9]};
    st16(hr0,c.rs,hv1,hsb); st16(hr1,c.rs,hv1,hsb+2048);
    { auto sx=__builtin_amdgcn_permlane16_swap(hr2.x,hr2.x,false,false); auto sy=__builtin_amdgcn_permlane16_swap(hr2.y,hr2.y,false,false);
      st16(v4u{sx[0],sy[0],sx[1],sy[1]},c.rs,hv2,hsb+4096); }
    float s1=0.f,s1b=0.f; for(int i=0;i<5;i++){ s1=dot2(hw[2*i],C11,s1); s1b=dot2(hw[2*i+1],C11,s1b); }
    for(int i=0;i<10;i++) xf[i]=f32x2{blo(hw[i]),bhi(hw[i])};
    s1=xh_add(x16_add(s1+s1b))*(1.f/80.f);
    SB0();
    for(int i=0;i<10;i++) xf[i]-=s1;
    SB0();
    f32x2 s2={0.f,0.f}, s3={0.f,0.f}; for(int i=0;i<5;i++){ s2=__builtin_elementwise_fma(xf[2*i],xf[2*i],s2); s3=__builtin_elementwise_fma(xf[2*i+1],xf[2*i+1],s3); }
    s2+=s3;
    float s2t=xh_add(x16_add(s2[0]+s2[1]));
    st8(v2u{__float_as_uint(s1),__float_as_uint(s2t)},c.rs,sv,ssb);
    stamp(ws,c.b,18);
  }
  vmwait<4>(); asm volatile("" : "+v"(wf0),"+v"(wf1));
  for(int sp=0;sp<SPIN_CAP;++sp){
    if(!__any(((wf0.x^c.tag.x)|(wf1.x^c.tag.x)|(wf0.y^c.tag.y)|(wf1.y^c.tag.y))!=0)) break;
    c.nretry+=1<<24;
    ld8(wf0,c.rs,wfo0,WS_FLAG+FK_W*256*128); ld8(wf1,c.rs,wfo1,WS_FLAG+FK_W*256*128);
    vmwait<0>(); asm volatile("" : "+v"(wf0),"+v"(wf1));
  }
  UNROLL(NPRE,I,issue_piece<I>(c));
  for(int i=0;i<3;i++){ c.lp[i]=lane*16 + i*65536; c.lq[i]=lane*16 + i*61440 + kh*5120; asm volatile("" : "+v"(c.lp[i]), "+v"(c.lq[i])); }
  c.actst = WS_ACT + ((pi*4+w)*2+blk)*55296 + hh*256 + tk*16; PIN(c.actst);
  c.actld = WS_ACT + (pi*8+rh*4+2*th)*55296 + kh*1024;
  c.av[0]=lane*16+kh*55296; c.av[1]=lane*16+(1-kh)*55296; PIN(c.av[0]); PIN(c.av[1]);
  v2u stt[4];
  c.xo = WS_H + (pi*4+w)*40960; c.rot=cc; c.pollv=(pi*16+tk)*128; PIN(c.pollv);
  unsigned stv=(4*hh)*1024+(32*w+tok)*8; PIN(stv);
  stamp(ws,c.b,19);
  auto window=[&]() AI {
    for(int sp=0;sp<SPIN_CAP;++sp){
      for(int q=0;q<4;q++) ld8(stt[q],c.rs,stv,WS_ST+pi*8192+q*1024);
      UNROLL(XPRE,I,xload<I>(c));
      vmwait<XPRE-XVER>(); UNROLL(XVER,I,pinA(c.X[I]));
      asm volatile("" : "+v"(stt[0]),"+v"(stt[1]),"+v"(stt[2]),"+v"(stt[3]));
      unsigned m=max(max(stt[0].x,stt[1].x),max(stt[2].x,stt[3].x));
      for(int i=0;i<XVER;i++) m=max(m,c.X[i].x);
      if(!__any(m==POISON)) break;
      c.nretry+=1;
      vmwait<0>();
    } };
  Epi ew;
  for(int sp=0;;++sp){
    window();
    if(sp==0) barrier();
    for(int r=0;r<RA;r++) c.A[r]=ldsA(c,r);
    { float m0=__uint_as_float(stt[0].x),m1=__uint_as_float(stt[1].x),m2=__uint_as_float(stt[2].x),m3=__uint_as_float(stt[3].x);
      float mean=xh_add((m0+m1)+(m2+m3))*0.125f;
      float q=(__uint_as_float(stt[0].y)+__uint_as_float(stt[1].y))+(__uint_as_float(stt[2].y)+__uint_as_float(stt[3].y));
      q += 80.f*(((m0-mean)*(m0-mean)+(m1-mean)*(m1-mean))+((m2-mean)*(m2-mean)+(m3-mean)*(m3-mean)));
      q = xh_add(q);
      c.var=q*(1.f/640.f)+1e-5f; float rs=__builtin_amdgcn_rsqf(c.var); c.c1r=GC1*rs; c.c2r=GC2*rs*rs*rs;
      unsigned mb=pk2(-mean,-mean); c.MB=v4u{mb,mb,mb,mb}; pinV(c.MB); PIN(c.var); PIN(c.c1r); PIN(c.c2r);
    }
    c.xmax=0;
    SB0();
    stamp(ws,c.b,20);
    UNROLL(G1,I,gapx<I>(c,ew));
    if(__builtin_expect(!__any(c.xmax==POISON) || sp>=REDO_CAP,1)) break;
    c.nretry+=1;
    vmwait<0>(); lgk0();
  }
  UNROLL(GN-G1,I,gapx<G1+I>(c,ew));
  stamp(ws,c.b,21);
  { f32x2 hf[10]; { unsigned hw[10]={hr0.x,hr0.y,hr0.z,hr0.w,hr1.x,hr1.y,hr1.z,hr1.w,hr2.x,hr2.y}; for(int i=0;i<10;i++) hf[i]=f32x2{blo(hw[i]),bhi(hw[i])}; }
    SB0();
    asm volatile("" : "+v"(c.acc4[1][0]),"+v"(c.acc4[1][1]),"+v"(c.acc4[1][2]),"+v"(c.acc4[1][3]),"+v"(c.acc4[1][4]));
    for(int u=0;u<5;u++) *(LAS f32x4*)(c.lp[0]+w*5120+u*1024) = c.acc4[1][u];
    asm volatile("" : "+v"(c.acc4[0][0]),"+v"(c.acc4[0][1]),"+v"(c.acc4[0][2]),"+v"(c.acc4[0][3]),"+v"(c.acc4[0][4]));
    for(int i=0;i<10;i++) hf[i]+=f32x2{c.acc4[0][i/2][2*(i&1)],c.acc4[0][i/2][2*(i&1)+1]};
    const v4u rout=mkrs(ar.out,S*D*2);
    lgk0(); barrier();
    f32x4 r[5]; for(int u=0;u<5;u++) r[u]=*(const LAS f32x4*)(c.lp[0]+(w^1)*5120+u*1024);
    unsigned ow[10];
    for(int i=0;i<10;i++){ f32x2 y=hf[i]+f32x2{r[i/2][2*(i&1)],r[i/2][2*(i&1)+1]}; ow[i]=pk2(y[0],y[1]); }
    const unsigned so=t16*20480+cc*160, vo=tk*1280+g4*16;
    asm volatile("buffer_store_dwordx4 %0, %1, %2, %3 offen" :: "v"(v4u{ow[0],ow[1],ow[2],ow[3]}), "v"(vo), "s"(rout), "s"(so) : "memory");
    asm volatile("buffer_store_dwordx4 %0, %1, %2, %3 offen offset:64" :: "v"(v4u{ow[4],ow[5],ow[6],ow[7]}), "v"(vo), "s"(rout), "s"(so) : "memory");
    asm volatile("buffer_store_dwordx2 %0, %1, %2, %3 offen offset:128\n\ts_nop 1" :: "v"(v2u{ow[8],ow[9]}), "v"(vo-g4*8), "s"(rout), "s"(so) : "memory");
  }
  if(lid==0 && w==0) st8(c.tag,c.rs,c.flagv,WS_EPOCH);
  asm volatile(".Lhg_code_end:");
  stamp(ws,c.b,22);
}

extern "C" void kernel_launch(void* const* d_in, const int* in_sizes, int n_in,
                              void* d_out, int out_size, void* d_ws, size_t ws_size,
                              hipStream_t stream) {
  Args a; a.x=(const u16*)d_in[0]; a.wqkv=(const u16*)d_in[1]; a.wphi=(const u16*)d_in[2]; a.wo=(const u16*)d_in[3];
  a.wg=(const u16*)d_in[5]; a.wu=(const u16*)d_in[6]; a.wd=(const u16*)d_in[7]; a.out=(u16*)d_out; a.ws=(char*)d_ws;
  hipFuncSetAttribute((const void*)blk_k, hipFuncAttributeMaxDynamicSharedMemorySize, 163840);
  for(int r=0;r<REPS;r++) blk_k<<<256,256,163840,stream>>>(a);
}
```

```cpp
#define RAV 6
#define FFST 0
#define SYL 200
#define PCD 28
#define HOTW 0
#define FS0 160
#define FS1 220
#define PAIRST 0
#define BARL 1
#define XPV 12
#define NPRE 30
#define WLD 310
#define TG1 35
#define TG3 38
#define TG4 37
#define TOP 70
#define QAL 280
#define PL0 203
#define PL1 250
#define PL2 28
#define PL3 58
#define XROT 1
#define AROT 0
constexpr int PW = 2;
constexpr int G3 = 280, GE = 288, GN = GE+135, NSTG = 26, RA = RAV, NA = RAV+2;
constexpr int G1 = 40*PW, EP0 = G1+4, EPL = 7, XPRE = XPV, XVER = 0;
enum { OP_NONE=0, OP_XL, OP_DMA, OP_ST, OP_WL, OP_AL, OP_PL, OP_CS };
constexpr bool is_sync(int g){ return g<G3 ? (g%(g<SYL? 40:20)==0 && g>=G1) : (g>=GE && g<GE+120 && (g-GE)%20==0); }
constexpr int sync_stage(int g){ return g<G3 ? g/20 : 14+(g-GE)/10; }
constexpr int sync_need(int g){ int m = g<G3 ? g/20+(g<SYL? 2:1) : 14+(g-GE)/10+2; return m<NSTG? m : NSTG-1; }
constexpr int ep_start(int t){ int g=EP0; for(int i=1;i<=t;i++){ g+=4*EPL; if(i>=PW && g<40*i+44) g=40*i+44; } return g; }
constexpr int PLG[4] = {PL0, PL1, GE+PL2, GE+PL3};
constexpr int PCG[4] = {PL0+PCD, PL1+PCD, GE+PL2+PCD, GE+PL3+PCD};
constexpr int FSG[4] = {FS0, FS1, GE, GE+20};
constexpr int FST[4] = {1, 3, 5, 6};
struct Sched { int op[GN+1]; int ix[GN+1]; int cnt[GN+2]; int dmag[NSTG]; int stg[7]; int xg[40]; int ag[54]; int wg[15]; int cg[7]; int ept[GN+1]; };
constexpr int al_kind(int i){ return i<48 ? i/16 : 3; }
constexpr int cs_use(int t){ return t<PW ? G1-PW+t : 40*t+39; }
constexpr Sched make_sched(){
  Sched s{};
  for(int g=0;g<=GN;g++) s.ept[g]=-1;
  for(int t=0;t<6;t++){ int g0=ep_start(t); for(int e=0;e<4*EPL;e++) s.ept[g0+e]=t*4*EPL+e; int g=g0+4*EPL; if(PAIRST && !(t&1)) g=ep_start(t+1)+4*EPL-1; s.op[g]=OP_ST; s.ix[g]=t; s.stg[t]=g; }
  s.op[GE-1]=OP_ST; s.ix[GE-1]=6; s.stg[6]=GE-1;
  for(int k=0;k<4;k++){ s.op[PLG[k]]=OP_PL; s.ix[PLG[k]]=k; }
  for(int m=0;m<NSTG;m++) s.dmag[m]=-1;
  for(int i=0;i<XPRE;i++) s.xg[i]=-1;
  int ddl[NSTG]={};
  for(int m=0;m<NSTG;m++) for(int q=0;q<GN;q++) if(is_sync(q) && sync_need(q)>=m){ ddl[m]=q; break; }
  int nx=XPRE, nd=NPRE, na=0, nw=0, nc=0, cur=0, now=0, busy=QAL+2*TOP;
  for(int g=0; g<GN; g++){
    if(is_sync(g)) cur=sync_stage(g);
    now += g<EP0 ? TG1 : g<G3 ? TG3 : g<GE ? 40 : TG4;
    if(busy<now) busy=now;
    if(s.op[g]!=OP_NONE){ busy+=34; continue; }
    if(busy-now>QAL) continue;
    int pick=OP_NONE;
    if(nx<40) pick=OP_XL;
    else {
      int dd=1<<20, da=1<<20, dc=1<<20;
      if(nd<NSTG*5 && nd/5<=cur+7) dd=ddl[nd/5];
      if(na<54 && g>=PCG[al_kind(na)]) da=GE+(na/2)*5;
      if(nc<7) dc=cs_use(nc);
      int dw = (nw<15 && g>=60) ? WLD : (1<<20);
      int mn = dd<da? dd:da; mn = mn<dc? mn:dc; mn = mn<dw? mn:dw;
      if(mn<(1<<20)) pick = mn==dc? OP_CS : mn==da? OP_AL : mn==dd? OP_DMA : OP_WL;
    }
    if(pick==OP_NONE) continue;
    s.op[g]=pick; busy+=TOP;
    if(pick==OP_XL){ s.ix[g]=nx; s.xg[nx]=g; nx++; }
    if(pick==OP_DMA){ s.ix[g]=nd; s.dmag[nd/5]=g; nd++; }
    if(pick==OP_AL){ s.ix[g]=na; s.ag[na]=g; na++; }
    if(pick==OP_WL){ s.ix[g]=nw; s.wg[nw]=g; nw++; }
    if(pick==OP_CS){ s.ix[g]=nc; s.cg[nc]=g; nc++; }
  }
  s.cnt[0]=0;
  for(int g=0;g<=GN;g++) s.cnt[g+1]=s.cnt[g]+(s.op[g]!=OP_NONE);
  return s;
}
constexpr Sched SC = make_sched();
constexpr bool sched_ok(){
  if(NPRE%5 || NPRE<25 || NPRE>35) return false;
  for(int g=0;g<GN;g++) if(is_sync(g) && SC.dmag[sync_need(g)]>=g) return false;
  for(int m=NPRE/5;m<NSTG;m++) if(SC.dmag[m]<0) return false;
  for(int s=XPRE;s<40;s++) if(SC.xg[s]>=PW*s-2) return false;
  for(int i=0;i<54;i++) if(SC.ag[i]<=0 || SC.ag[i]>=GE+(i/2)*5-1) return false;
  for(int i=0;i<15;i++) if(SC.wg[i]<=0 || SC.wg[i]>=SC.ag[49]) return false;
  for(int i=0;i<7;i++) if(SC.cg[i]<=0 || SC.cg[i]>=cs_use(i)) return false;
  for(int k=0;k<4;k++) if(SC.stg[FST[k]]>=FSG[k] || !is_sync(FSG[k]) || SC.op[PLG[k]]!=OP_PL) return false;
  for(int t=0;t<7;t++) if(SC.op[SC.stg[t]]!=OP_ST) return false;
  if(SC.stg[5]>=G3) return false;
  return true;
}
static_assert(sched_ok(), "schedule");
constexpr int after(int g, int g0){ return SC.cnt[g]-SC.cnt[g0+1]; }

struct Ctx {
  v4u rs; char* ws; unsigned lp[3], lq[3]; unsigned voff; int lane, w, b, pi, pj; v2u tag;
  unsigned src3, src4, actst, actld, av[2], xo, wlo, pollv, flagv, rot, xmax;
  v4u X[40]; v4u CS[7]; v4u WL[15]; v4u MB; v4u A[NA];
  f32x16 acc[7]; f32x4 acc4[2][5]; v4u B[54]; v2u pf;
  float c1r, c2r, var; float t0,t1,e0,e1,v0,v1; unsigned pk[2][4];
  unsigned nretry; u64 stt; unsigned stv;
};
constexpr int SLOT[8]={5,6,0,1,2,7,3,4};
constexpr int fpos(int f){ return SLOT[(f/20)%8]*20+f%20; }
DI v4u ldsA(const Ctx& c, int a){
  if(a<G3) return *(const LAS v4u*)(c.lp[fpos(a)/64] + (fpos(a)%64)*1024);
  int q=a-G3; int f=fpos(G3 + (q/5)*10 + q%5);
  return *(const LAS v4u*)(c.lq[f/60] + (f%60)*1024);
}
template<int M> DI void issue_piece(Ctx& c){
  constexpr int stg=M/5, p=M%5, pb = p<4 ? 0 : 4;
  unsigned lds = SLOT[stg%8]*20480 + pb*1024 + c.w*5120;
  unsigned so = (HOTW? c.src3 + (stg%3)*20480 : stg<14 ? c.src3 + stg*20480 : c.src4 + (stg-14)*20480) + pb*1024;
  if constexpr(stg==12 || stg==13) so = c.pj>=12 ? WS_END : so;
  if constexpr(p==pb) dma1(c.rs, lds, c.voff, so);
  else asm volatile("buffer_load_dwordx4 %0, %1, %2 offen offset:%3 sc1 lds" :: "v"(c.voff), "s"(c.rs), "s"(so), "n"(p*1024) : "memory");
}
constexpr float GC1 = -2.302208198f, GC2 = -0.1029432397f;
#define PIN(x) asm volatile("" : "+v"(x))
template<int T,int P,int STEP> DI void epi7(Ctx& c){
  constexpr int q=2*P;
  if constexpr(STEP==0){ c.t0=c.acc[T][q]*c.acc[T][q]; PIN(c.t0); c.t1=c.acc[T][q+1]*c.acc[T][q+1]; PIN(c.t1); c.v0=c.acc[T][q]*c.acc[T][q+8]; PIN(c.v0); }
  if constexpr(STEP==1){ c.t0=__builtin_fmaf(c.t0,c.c2r,c.c1r); PIN(c.t0); c.t1=__builtin_fmaf(c.t1,c.c2r,c.c1r); PIN(c.t1); c.v1=c.acc[T][q+1]*c.acc[T][q+9]; PIN(c.v1); }
  if constexpr(STEP==2){ c.t0*=c.acc[T][q]; PIN(c.t0); c.t1*=c.acc[T][q+1]; PIN(c.t1); }
  if constexpr(STEP==3){ c.e0=__builtin_amdgcn_exp2f(c.t0); PIN(c.e0); c.e1=__builtin_amdgcn_exp2f(c.t1); PIN(c.e1); }
  if constexpr(STEP==4){ c.e0=__builtin_fmaf(c.e0,c.var,c.var); PIN(c.e0); c.e1=__builtin_fmaf(c.e1,c.var,c.var); PIN(c.e1); }
  if constexpr(STEP==5){ c.e0=__builtin_amdgcn_rcpf(c.e0); PIN(c.e0); c.e1=__builtin_amdgcn_rcpf(c.e1); PIN(c.e1); }
  if constexpr(STEP==6){ c.v0*=c.e0; PIN(c.v0); c.v1*=c.e1; PIN(c.v1); c.pk[T&1][P]=pk2(c.v0,c.v1); PIN(c.pk[T&1][P]); }
}
struct Epi { f32x2 t[4], v[4]; };
template<int STEP> DI void epiw(Ctx& c, Epi& e){
  for(int i=0;i<4;i++){ const f32x2 a={c.acc[6][2*i],c.acc[6][2*i+1]};
    if constexpr(STEP==0){ e.t[i]=a*a; e.v[i]=a*f32x2{c.acc[6][2*i+8],c.acc[6][2*i+9]}; }
    if constexpr(STEP==1) e.t[i]=__builtin_elementwise_fma(e.t[i],f32x2{c.c2r,c.c2r},f32x2{c.c1r,c.c1r});
    if constexpr(STEP==2) e.t[i]*=a;
    if constexpr(STEP==3) e.t[i]=f32x2{__builtin_amdgcn_exp2f(e.t[i][0]),__builtin_amdgcn_exp2f(e.t[i][1])};
    if constexpr(STEP==4) e.t[i]=__builtin_elementwise_fma(e.t[i],f32x2{c.var,c.var},f32x2{c.var,c.var});
    if constexpr(STEP==5) e.t[i]=f32x2{__builtin_amdgcn_rcpf(e.t[i][0]),__builtin_amdgcn_rcpf(e.t[i][1])};
    if constexpr(STEP==6) e.v[i]*=e.t[i];
    if constexpr(STEP==7) c.pk[0][i]=pk2(e.v[i][0],e.v[i][1]); }
  if constexpr(STEP<7) asm volatile("" : "+v"(e.t[0]),"+v"(e.t[1]),"+v"(e.t[2]),"+v"(e.t[3]),"+v"(e.v[0]),"+v"(e.v[1]),"+v"(e.v[2]),"+v"(e.v[3]));
}
template<int T> DI void act_store(Ctx& c){
  unsigned so = T<6 ? (16*(T>>1))*1024 + (T&1)*512 + c.pj*1024 : 48*1024 + (c.pj>>1)*1024 + (c.pj&1)*512;
  if(T==6 && c.pj>=12) so = WS_END;
  st16(v4u{c.pk[T&1][0],c.pk[T&1][1],c.pk[T&1][2],c.pk[T&1][3]}, c.rs, c.actst, so);
}
template<int ix> DI void xload(Ctx& c){
  if constexpr(XROT){ constexpr int e=ix%5; ldA<(e&3)*1024>(c.X[ix], c.rs, c.voff, c.xo + ((ix/5+c.rot)&7)*5120 + (e&4)*1024); }
  else ldA<(ix%4)*1024>(c.X[ix], c.rs, c.voff, c.xo + (ix/4)*4096); }
template<int ix> DI void aload(Ctx& c){ constexpr int m=ix/2;
  if constexpr(AROT && m<24) ldV<0>(c.B[ix], c.rs, c.av[ix&1], c.actld + (m/8)*16384+((m+c.rot)&7)*2048);
  else ldV<(m&1)*2048>(c.B[ix], c.rs, c.av[ix&1], c.actld + (m/2)*4096); }
template<int g> DI void vmem_op(Ctx& c){
  constexpr int op=SC.op[g], ix=SC.ix[g];
  if constexpr(op==OP_XL) xload<ix>(c);
  if constexpr(op==OP_DMA) issue_piece<ix>(c);
  if constexpr(op==OP_ST) act_store<ix>(c);
  if constexpr(op==OP_WL) ldA<((ix%5)&3)*1024>(c.WL[ix], c.rs, c.voff, c.wlo + (ix/5)*10240 + ((ix%5)&4)*1024);
  if constexpr(op==OP_CS) ldA<(ix%4)*1024>(c.CS[ix], c.rs, c.voff, (ix==6 && c.pj>=12)? WS_END : WS_CS3 + c.pj*8192 + (ix/4)*4096);
  if constexpr(op==OP_AL) aload<ix>(c);
  if constexpr(op==OP_PL) ld8(c.pf, c.rs, c.pollv, WS_FLAG+(FK_D0+ix)*256*128);
}
constexpr unsigned POISON = 0xffffffffu;
DI void useA(v4u a, v4u b, v4u c, v4u d){ asm volatile("" :: "v"(a),"v"(b),"v"(c),"v"(d)); }
template<int g> DI void sync_pt(Ctx& c){
  if constexpr(is_sync(g)){
    constexpr int m=sync_need(g);
    constexpr int n1 = SC.dmag[m]>=0 ? after(g,SC.dmag[m]) : 63;
    constexpr int k = g==FSG[0]?0 : g==FSG[1]?1 : g==FSG[2]?2 : g==FSG[3]?3 : -1;
    constexpr int n2 = k>=0 ? after(g,SC.stg[FST[k<0?0:k]]) : 63;
    if constexpr((n1<n2?n1:n2)<63) vmwait<(n1<n2?n1:n2)>();
    barrier();
    if constexpr(k>=0){ if(c.w==k) st8(c.tag, c.rs, c.flagv, WS_FLAG+(FK_D0+k)*256*128); }
    SB0();
  }
}
template<int g> DI void gapx(Ctx& c, Epi& ew){
  if constexpr(!BARL) sync_pt<g>(c);
  if constexpr(g<G3){
    constexpr int T = g<G1 ? g%PW : g/40, s = g<G1 ? g/PW : g%40;
    if constexpr(g%4==0) useA(c.A[g%NA],c.A[(g+1)%NA],c.A[(g+2)%NA],c.A[(g+3)%NA]);
    if constexpr(s==0) mfma32_0(c.acc[T], c.A[g%NA], c.X[s]); else mfma32(c.acc[T], c.A[g%NA], c.X[s]);
    if constexpr(s==39){ vmwait<after(g,SC.cg[T])>(); mfma32av(c.acc[T], c.CS[T], c.MB); }
    SB0();
    if constexpr(BARL) sync_pt<g>(c);
    c.A[(g+RA)%NA] = ldsA(c, g+RA);
    vmem_op<g>(c);
    if constexpr(g<G1 && s>=XVER && T==0){ pinA(c.X[s]); c.e0=__uint_as_float(c.X[s].x); PIN(c.e0); }
    if constexpr(g<G1 && s>=XVER && T==1){ c.xmax=max(c.xmax,__float_as_uint(c.e0)); PIN(c.xmax); }
    if constexpr(g<G1 && T==PW-1 && s+1>=XVER && s+1<40){ if constexpr(s+1<XPRE) vmwait<XPRE-2-s+SC.cnt[g+1]>(); else vmwait<after(g+1,SC.xg[s+1])>(); }
    if constexpr(SC.ept[g]>=0){ constexpr int e=SC.ept[g]; epi7<e/(4*EPL),(e/EPL)%4,e%EPL>(c); }
  } else if constexpr(g<GE){
    if constexpr(g==G3) asm volatile("s_nop 9" : "+v"(c.acc[6]));
    epiw<g-G3>(c,ew);
    vmem_op<g>(c);
  } else {
    constexpr int q=g-GE, m=q/5, u=q%5, a=G3+q;
    if constexpr(q<120 && q%4==0) useA(c.A[a%NA],c.A[(a+1)%NA],c.A[(a+2)%NA],c.A[(a+3)%NA]);
    if constexpr(q<120){
      if constexpr(m==0) mfma16_0(c.acc4[1][u], c.A[a%NA], c.B[1]); else mfma16(c.acc4[1][u], c.A[a%NA], c.B[2*m+1]);
      SB0();
      if constexpr(BARL) sync_pt<g>(c);
      vmem_op<g>(c);
      SB0();
      if constexpr(m==0) mfma16_0(c.acc4[0][u], c.A[a%NA], c.B[0]); else mfma16(c.acc4[0][u], c.A[a%NA], c.B[2*m]);
    } else { mfma16a(c.acc4[1][u], c.WL[q-120], c.B[2*m+1]); SB0(); vmem_op<g>(c); SB0(); mfma16a(c.acc4[0][u], c.WL[q-120], c.B[2*m]); }
    SB0();
    if constexpr(q+RA<120) c.A[(a+RA)%NA] = ldsA(c, a+RA);
  }
  if constexpr(g>=GE-1 && (g+1-GE)%5==0 && g+1<GN) vmwait<after(g+1,SC.ag[2*((g+1-GE)/5)+1])>();
  { constexpr int k = g+1==PCG[0]?0 : g+1==PCG[1]?1 : g+1==PCG[2]?2 : g+1==PCG[3]?3 : -1;
    if constexpr(k>=0){
      vmwait<after(g+1,PLG[k])>(); asm volatile("" : "+v"(c.pf));
      if(__builtin_expect(__any(((c.pf.x^c.tag.x)|(c.pf.y^c.tag.y))!=0),0)){ c.nretry+=256<<(4*k);
        for(int sp=0;sp<SPIN_CAP;++sp){ ld8(c.pf, c.rs, c.pollv, WS_FLAG+(FK_D0+k)*256*128); vmwait<0>(); asm volatile("" : "+v"(c.pf)); if(!__any(((c.pf.x^c.tag.x)|(c.pf.y^c.tag.y))!=0)) break; } }
    } }
  SB0();
}
#define MAPM 8
#define OL0 10
#define REPS 1
#define RP0 8
#define OROT 1
#define PZC 0
#define C0A 6
#define RP2 8
#define NOCT 0
#define RP3 5
#define RP4 10
#define KL0 0
#define ODLY 0
#define PSLP 2
#define NOST 0
#define TLBT 0
#define ROT 1
constexpr int REDO_CAP=64;
#define BSM 0
constexpr int BS[6][11]={{0,2,4,6,8,10,12,14,14,14,14},{0,0,5,5,9,9,14,14,14,14,14},{0,0,0,7,7,7,14,14,14,14,14},{0,0,0,0,14,14,14,14,14,14,14},{0,0,0,0,0,0,0,14,14,14,14},{0,0,0,0,0,0,7,14,14,14,14}};
constexpr int bs0(int kc){ return BS[BSM][kc<0?0:kc]; }
template<int I> using IC = std::integral_constant<int,I>;
template<int N,int DP,class RD,class MM,class SD> DI void pipe(RD rd, MM mm, SD sd){
  v4u A[DP+1];
  [&]<int... I>(std::integer_sequence<int,I...>) AI { ((A[I]=rd(IC<I>{})), ...); }(std::make_integer_sequence<int,DP>{});
  SB0();
  auto step=[&](auto i) AI { constexpr int I=i; if constexpr(I%2==0 && I+1<N) asm volatile("" :: "v"(A[I%(DP+1)]),"v"(A[(I+1)%(DP+1)])); mm(i,A[I%(DP+1)]); SB0(); sd(i); if constexpr(I+DP<N) A[(I+DP)%(DP+1)]=rd(IC<I+DP>{}); SB0(); };
  [&]<int... I>(std::integer_sequence<int,I...>) AI { (step(IC<I>{}), ...); }(std::make_integer_sequence<int,N>{});
}
DI void mfma32vv_0(f32x16& c, v4u a, v4u x){ asm volatile("v_mfma_f32_32x32x16_bf16 %0, %1, %2, 0" : "=&v"(c) : "v"(a), "v"(x)); }
DI void mfma32vv(f32x16& c, v4u a, v4u x){ asm volatile("v_mfma_f32_32x32x16_bf16 %0, %1, %2, %0" : "+v"(c) : "v"(a), "v"(x)); }
DI void mfma32A_0(f32x16& c, v4u a, v4u x){ asm volatile("v_mfma_f32_32x32x16_bf16 %0, %1, %2, 0" : "=a"(c) : "v"(a), "v"(x)); }
DI void mfma32A(f32x16& c, v4u a, v4u x){ asm volatile("v_mfma_f32_32x32x16_bf16 %0, %1, %2, %0" : "+a"(c) : "v"(a), "v"(x)); }
DI void mfma32av_0(f32x16& c, v4u a, v4u x){ asm volatile("v_mfma_f32_32x32x16_bf16 %0, %1, %2, 0" : "=&v"(c) : "a"(a), "v"(x)); }
DI void mfma16a_0(f32x4& c, v4u a, v4u x){ asm volatile("v_mfma_f32_16x16x32_bf16 %0, %1, %2, 0" : "=&v"(c) : "a"(a), "v"(x)); }
DI void nop2(){ asm volatile("s_nop 1" ::: "memory"); }
DI void mdone(f32x16& c){ asm volatile("s_nop 11" : "+v"(c)); }
#define PINS(x) asm volatile("" : "+s"(x))
DI float max3(float a,float b,float c){ float r; asm("v_max3_f32 %0, %1, %2, %3" : "=v"(r) : "v"(a),"v"(b),"v"(c)); return r; }
DI float min3(float a,float b,float c){ float r; asm("v_min3_f32 %0, %1, %2, %3" : "=v"(r) : "v"(a),"v"(b),"v"(c)); return r; }
DI f32x2 xh(float v){ auto r=__builtin_amdgcn_permlane32_swap(__float_as_uint(v),__float_as_uint(v),false,false); return f32x2{__uint_as_float(r[0]),__uint_as_float(r[1])}; }
template<class H> DI void hedgehog2(f32x16 p, f32x16 r, bool hh, float sc, unsigned (&o)[2][10], H hook){
  f32x16 z[2]={p,r};
  float a3[2][3], i3[2][3], mx[2], mn[2];
  for(int a=0;a<2;a++){ float x8=z[a][8],x9=z[a][9],xa=z[a][10],xb=z[a][11]; PIN(xa); PIN(xb); z[a][8]=hh? xa:x8; z[a][9]=hh? xb:x9; }
  for(int a=0;a<2;a++) for(int k=0;k<2;k++){ a3[a][k]=max3(z[a][3*k],z[a][3*k+1],z[a][3*k+2]); i3[a][k]=min3(z[a][3*k],z[a][3*k+1],z[a][3*k+2]); }
  SB0();
  for(int a=0;a<2;a++){ a3[a][2]=max3(z[a][6],z[a][7],z[a][8]); i3[a][2]=min3(z[a][6],z[a][7],z[a][8]); }
  for(int a=0;a<2;a++){ mx[a]=max3(a3[a][0],a3[a][1],z[a][9]); mn[a]=min3(i3[a][0],i3[a][1],z[a][9]); }
  SB0();
  for(int a=0;a<2;a++){ mx[a]=max3(mx[a],a3[a][2],a3[a][2]); mn[a]=min3(mn[a],i3[a][2],i3[a][2]); }
  SB0();
  f32x2 sx[2], sn_[2];
  for(int a=0;a<2;a++){ sx[a]=xh(mx[a]); sn_[a]=xh(mn[a]); }
  SB0();
  for(int a=0;a<2;a++){ mx[a]=max3(sx[a][0],sx[a][1],sx[a][1]); mn[a]=min3(sn_[a][0],sn_[a][1],sn_[a][1]); }
  SB0();
  for(int a=0;a<2;a++){ mx[a]*=-sc; mn[a]*=sc; }
  SB0(); hook(0); SB0();
  f32x2 ep[2][5], en[2][5];
  for(int a=0;a<2;a++) for(int i=0;i<5;i++){ f32x2 v={z[a][2*i],z[a][2*i+1]};
      ep[a][i]=__builtin_elementwise_fma(v,f32x2{sc,sc},f32x2{mx[a],mx[a]}); en[a][i]=__builtin_elementwise_fma(v,f32x2{-sc,-sc},f32x2{mn[a],mn[a]}); }
  SB0();
  for(int a=0;a<2;a++){
    for(int i=0;i<5;i++){ ep[a][i]=f32x2{__builtin_amdgcn_exp2f(ep[a][i][0]),__builtin_amdgcn_exp2f(ep[a][i][1])}; en[a][i]=f32x2{__builtin_amdgcn_exp2f(en[a][i][0]),__builtin_amdgcn_exp2f(en[a][i][1])}; }
    asm volatile("" : "+v"(ep[a][0]),"+v"(ep[a][1]),"+v"(ep[a][2]),"+v"(ep[a][3]),"+v"(ep[a][4]),"+v"(en[a][0]),"+v"(en[a][1]),"+v"(en[a][2]),"+v"(en[a][3]),"+v"(en[a][4]));
    SB0(); hook(1+a); SB0(); }
  f32x2 s0[4], s1[4];
  for(int a=0;a<2;a++){ s0[a]=ep[a][0]+ep[a][1]; s1[a]=ep[a][2]+ep[a][3]; s0[2+a]=en[a][0]+en[a][1]; s1[2+a]=en[a][2]+en[a][3]; }
  SB0();
  for(int a=0;a<2;a++){ s0[a]+=ep[a][4]; s0[2+a]+=en[a][4]; }
  SB0();
  for(int k=0;k<4;k++) s0[k]+=s1[k];
  SB0();
  float t[4]; for(int k=0;k<4;k++) t[k]=s0[k][0]+s0[k][1];
  SB0();
  for(int k=0;k<4;k++) s0[k]=xh(t[k]);
  SB0();
  for(int k=0;k<4;k++) t[k]=s0[k][0]+s0[k][1];
  SB0();
  for(int k=0;k<4;k++) t[k]=__builtin_amdgcn_rcpf(t[k]);
  SB0(); hook(3); SB0();
  for(int a=0;a<2;a++) for(int i=0;i<5;i++){ ep[a][i]*=t[a]; en[a][i]*=t[2+a]; }
  SB0();
  for(int a=0;a<2;a++) for(int i=0;i<5;i++){ o[a][i]=pk2(ep[a][i][0],ep[a][i][1]); o[a][5+i]=pk2(en[a][i][0],en[a][i][1]); }
  SB0();
}
DI v4u pack8r(const f32x16& a, int r0){ return v4u{pk2(a[r0],a[r0+1]),pk2(a[r0+2],a[r0+3]),pk2(a[r0+4],a[r0+5]),pk2(a[r0+6],a[r0+7])}; }

struct Args { const u16 *x,*wqkv,*wphi,*wo,*wg,*wu,*wd; u16* out; char* ws; };
constexpr unsigned M_W=0, M_XS=65536, M_RT=114688, M_PHI=79872, M_VI=M_PHI+128*168, M_WO=0;
constexpr unsigned M_R4=143360, M_DUM=M_RT+20480;
constexpr unsigned M_CSX=L_CSX, M_KVI=L_KVI, M_WP=L_KVI+4224;
template<int OFF> DI void dmaN(v4u rs, unsigned voff, unsigned soff){
  asm volatile("buffer_load_dwordx4 %0, %1, %2 offen offset:%3 lds" :: "v"(voff), "s"(rs), "s"(soff), "n"(OFF) : "memory");
}
DI void dma0nt(v4u rs, unsigned lds, unsigned voff, unsigned soff){
  asm volatile("s_mov_b32 m0, %0\n\ts_nop 0\n\tbuffer_load_dwordx4 %1, %2, %3 offen nt lds" :: "s"(lds), "v"(voff), "s"(rs), "s"(soff) : "memory");
}
template<int OFF> DI void dmaNnt(v4u rs, unsigned voff, unsigned soff){
  asm volatile("buffer_load_dwordx4 %0, %1, %2 offen offset:%3 nt lds" :: "v"(voff), "s"(rs), "s"(soff), "n"(OFF) : "memory");
}
DI void st16a(v4u d, v4u rs, unsigned voff, unsigned soff){
  asm volatile("buffer_store_dwordx4 %0, %1, %2, %3 offen sc1" :: "a"(d), "v"(voff), "s"(rs), "s"(soff) : "memory");
}
DI unsigned sel(bool c, unsigned a, unsigned b){ return c? a:b; }
DI void touch(unsigned& d, v4u rs, unsigned voff, unsigned soff){
  asm volatile("buffer_load_dword %0, %1, %2, %3 offen" : "=v"(d) : "v"(voff), "s"(rs), "s"(soff) : "memory");
}

__global__ __launch_bounds__(256,1) void blk_k(Args ar){
  extern __shared__ __attribute__((aligned(16))) char smem[];
  char* ws=ar.ws;
  Ctx c;
  c.ws=ws; c.b=blockIdx.x; c.lane=threadIdx.x&63; c.w=__builtin_amdgcn_readfirstlane(threadIdx.x>>6);
  c.pi=((c.b&7)>>2)*8+((c.b>>3)&7); c.pj=(c.b&3)*4+(c.b>>6);
  const int lane=c.lane, w=c.w, pi=c.pi, pj=c.pj, jq=pj^1, tid=threadIdx.x;
  c.stt=0; c.stv=0;
  stamp(ws,c.b,0);
  unsigned dummy;
  { u64 pc; unsigned len; asm volatile("s_getpc_b64 %0\n.Lhg_code_begin:\n\ts_mov_b32 %1, .Lhg_code_end-.Lhg_code_begin" : "=s"(pc), "=s"(len));
    touch(dummy, mkrs((const void*)pc,NOCT? 0:len), sel(lane<4,lane*128,OOB), ((c.b>>3)*16+w*4)*128); }
  const int lid=pi*16+pj; const bool job = lid<216;
  const int jp=(lid*152)>>12, jl=lid-jp*27, ju=lid*2+(w>>1), js=ju>>3;
  { const v4u rg=mkrs(w&1? ar.wu:ar.wg,job? D*F*2:0);
    const unsigned s3o=(jp*80+(w>>1)*40)*3456+jl*128, rtv=(lane>>3)*3456+(lane&7)*16;
    dma0nt(rg,M_RT+(w&1)*10240+(w>>1)*5120,rtv,s3o);
    dmaNnt<1024>(rg,rtv,s3o+(8*3456-1024)); dmaNnt<2048>(rg,rtv,s3o+2*(8*3456-1024)); dmaNnt<3072>(rg,rtv,s3o+3*(8*3456-1024));
    dma0nt(rg,M_RT+(w&1)*10240+(w>>1)*5120+4096,rtv,s3o+32*3456); }
  const v4u rx=mkrs(ar.x,S*D*2), rw=mkrs(ar.wqkv,D*1920*2);
  int kph = ROT==1? (3*pi+pj)%10 : ROT==2? 5*((pi+pj)&1) : ROT==3? ((pi+pj)&3)*5/2 : ROT==4? (pi&3)*5/2 : ROT==5? ((pi&3)+(pj&1)*5)%10 : ROT==6? (pi+3*pj)%10 : ROT==7? ((pi&7)+((pj&3)*5)/2)%10 : ROT==8? (((pi&3)+(pj&3))&3)*5/2 : ROT==9? ((pi+pj)%5)*2 : 0;
  const unsigned xs=M_XS+w*12288, xrow=(pi*128+w*32)*1280, wrow=w*16*3840;
  const int g4=lane>>4, tk=lane&15;
  unsigned xv[4]; for(int e=0;e<4;e++) xv[e]=(lane>>3)*1280+(((lane&7)^(g4+4*(e&1))))*16+e*(10240-1024);
  unsigned wv[4];
  { const unsigned ch=(tk-4*g4)&15; unsigned cb=sel(ch<5,1280+pj*80,2480+pj*80); cb=sel(ch<10,cb,jq*80-160); cb=sel(ch<15,cb,OOB);
    wv[0]=g4*3840+ch*16+cb; for(int p=1;p<4;p++) wv[p]=wv[0]+p*(15360-1024); }
  unsigned xso=xrow+kph*128, wso=wrow+kph*(64*3840);
  auto ldp=[&](int kc,auto P) AI { constexpr int p=P;
    if constexpr(p==0) dma0(rx, xs+(kc%3)*4096, xv[0], xso); else if constexpr(p<4) dmaN<p*1024>(rx,xv[p&3],xso);
    else if constexpr(p==4) dma0(rw, M_W+(kc%4)*16384+w*4096, wv[0], wso); else dmaN<(p-4)*1024>(rw,wv[(p-4)&3],wso);
    if constexpr(p==7){ kph = kph==9? 0:kph+1; xso=xrow+kph*128; wso=wrow+kph*(64*3840); } };
  UNROLL(C0A,I,ldp(0,IC<I>{}));
  SB0();
  c.rs=mkrs(ws,WS_END); c.voff=lane*16; c.nretry=0;
  const int tok=lane&31, hh=lane>>5;
  const int cc=pj&7, rh=pj>>3, th=w>>1, kh=w&1, t16=pi*8+rh*4+w;
  const int blk=g4&1, q4=tk>>2, p4=lane&3, qd=blk*4+p4;
  v4u PZ={POISON,POISON,POISON,POISON}; pinV(PZ);
  const unsigned osb=WS_O+(pi*4+w)*40960+jq*1280, hsb=WS_H+(t16>>1)*40960+cc*5120+(t16&1)*256, ssb=WS_ST+pi*8192+cc*1024+(rh*64+w*16)*8;
  unsigned ov=blk*20480+tk*16, ov1=ov+hh*256, ov2=sel(hh,OOB,ov+512), kvo=sel(tid<220,tid*16,OOB);
  unsigned hv1=tk*16+g4*512, hv2=sel(blk,OOB,tk*16+hh*512), sv=sel(lane<16,tk*8,OOB);
  unsigned pfo=sel(lane<16,tk*16+jq,pi*16+tk)*128, xo_=tk*1280+g4*16, xo2=xo_-g4*8;
  c.flagv=sel(lane==0,lid*128,OOB); PIN(c.flagv);
  PIN(ov1); PIN(ov2); PIN(kvo); PIN(hv1); PIN(hv2); PIN(sv); PIN(pfo); PIN(xo_); PIN(xo2);
  float sm[2]={0.f,0.f}, sq[2]={0.f,0.f};
  unsigned C11=0x3f803f80u; PIN(C11);
  unsigned xr[4]; { unsigned b0=xs+tok*128, x0=(hh^((tok>>1)&7))*16; for(int s=0;s<4;s++){ xr[s]=b0+(x0^(32*s)); PIN(xr[s]); } }
  v4u ONE; { unsigned o1=sel(tok==0,0x3f803f80u,0u); ONE=v4u{o1,o1,o1,o1}; pinV(ONE); }
  unsigned ab[4], aw[4];
  { const unsigned rb=M_W+256*(8*hh+q4), rq=8*q4; const bool lo=qd<5;
    ab[0]=rb+8*((rq+qd+sel(lo,0,8))&31); ab[1]=rb+8*((rq+qd+5)&31); ab[2]=rb+8*((rq+qd+sel(lo,20,12))&31); ab[3]=rb+8*((rq+sel(lo,25+qd,16))&31);
    for(int t=0;t<4;t++){ aw[t]=ab[t]+w*4096; PIN(ab[t]); PIN(aw[t]); } }
  const unsigned qd4=8*min(qd,4);
  unsigned pwb=M_WP+q4*40+qd4, pwa=pwb+hh*160; PIN(pwa); PIN(pwb);
  unsigned csw=sel(hh,M_DUM,M_CSX)+lane*16+w*4, csr=M_CSX+tok*16; PIN(csw); PIN(csr);
  unsigned prow=M_PHI+(w*32+tok)*168+hh*40, vrow=M_VI+(w*32+tok)*104; PIN(prow);
  unsigned vwa[4], vwb[2];
  vwa[0]=vrow+40-16*hh; vwa[1]=vrow+88-88*hh; vwa[2]=vrow+40+24*hh; vwa[3]=vrow+88-32*hh;
  vwb[0]=vrow+32+16*hh; vwb[1]=vrow+8+8*hh;
  for(int t=0;t<4;t++) PIN(vwa[t]); for(int t=0;t<2;t++) PIN(vwb[t]);
  unsigned pa=M_PHI+(8*hh+q4)*168+w*40+qd4, va=M_VI+(8*hh+q4)*104+(w>>1)*48+8*min(qd,5), kvs=sel(tok<22,tok*8+hh*176,OOB), kvs2=sel(hh,OOB,kvs);
  PIN(pa); PIN(va); PIN(kvs); PIN(kvs2);
  *(LAS v2u*)(sel(tid<88,M_KVI+tid*48+40,M_DUM))=v2u{0,0};
  v4u ro=mkrs(ar.wo,D*D*2), rd=mkrs(ar.wd,job? D*F*2:0); PINS(ro); PINS(rd);
  unsigned s4o=(js*32+(w&1)*16)*1280+(ju&7)*160, rdv[3];
  for(int p=0;p<3;p++){ int idx=p*64+lane, n=(idx*205)>>11; rdv[p]=n*(1280-160)+lane*16; PIN(rdv[p]); }
  PINS(s4o);
  auto rtp=[&](auto P) AI { constexpr int p=P;
    if constexpr(p==0) dma0nt(rd,M_R4+w*2560,rdv[0],s4o); else if constexpr(p==1) dmaNnt<1024>(rd,rdv[1],s4o); else { if(lane<32) dmaNnt<2048>(rd,rdv[2],s4o); } };
  { const int l3=(lane>>3)&3; unsigned ln=sel(l3==0,(6*pj)>>2,(6*pj+5)>>2); ln=sel(l3>=2 && pj<12,24+(pj>>2),ln); pfo=sel(hh,((lane&7)*27+ln)*128,pfo); PIN(pfo); }
  unsigned wov= MAPM==8? sel(lane<25,((lane*52)>>8)*640+lane*128,OOB) : sel(lane<50,lane*128,OOB); PIN(wov);
  unsigned ovv[5];
  for(int p=0;p<5;p++){ int idx=p*64+lane, n=(idx*205)>>11; int row=(n&~12)|((n&4)<<1)|((n&8)>>1); ovv[p]=row*1280-n*160+lane*16+(p&4)*1024; PIN(ovv[p]); }
  const int orot=OROT? (5*cc)>>1 : 0;
  auto wop=[&](auto N) AI { constexpr int n=N, p=n%5; constexpr int rn=n/5; int s= rn==0? 8+w : rn==1? (w==3? 7:w) : rn==2? 3+w : 4*rn+w, sp=s+orot; sp= sp>=20? sp-20:sp; unsigned so=sp*32*1280+cc*160;
    if constexpr(p%4==0) dma0(ro,M_WO+s*5120+p*1024,ovv[p],so); else dmaN<p*1024>(ro,ovv[p],so); };
  SB0();
  stamp(ws,c.b,27);
  UNROLL(8-C0A,I,ldp(0,IC<C0A+I>{}));
  v2u ep; ld8(ep,c.rs,0u,WS_EPOCH);
  unsigned dum2;
  if constexpr(TLBT){ if(w<2) touch(dum2,c.rs,lane*TLBT,w*64*TLBT+c.b*128); else touch(dum2,w==2? mkrs(ar.out,S*D*2):mkrs(ar.wd,D*F*2),lane*65536,c.b*128); }
  if(lane<50) dma0(mkrs(ar.wphi,NH*400*2), M_WP+w*800, c.voff, (pj>>1)*3200+w*800);
  v4u rf[5]; unsigned rcp_, d3, d3s, dcs, csv=sel(hh,OOB,tok*16); PIN(csv);
  { const unsigned r3=M_RT+blk*10240+(8*hh+q4)*128+8*(((p4&1)<<1)|(p4>>1))+w*32;
    const int tau=jl*4+w, j2= tau<96? (tau*43)>>8 : tau-96, i2= tau<96? tau-j2*6 : 6;
    const int sl0=(jp-(XROT? (j2&7):0))&7;
    d3= job? WS_WT3+j2*286720+(i2<PW? PW*sl0*5+i2 : 40*i2+sl0*5)*1024 : WS_END; d3s= i2<PW? PW*1024:1024;
    dcs=job? WS_CS3+j2*8192+i2*1024+(jp>>2)*512+(jp&3)*4:WS_END;
    PINS(d3); PINS(d3s); PINS(dcs);
    vmwait<(TLBT? 11:10)>(); barrier();
    stamp(ws,c.b,15);
    for(int s=0;s<5;s++) rf[s]=trfrag(r3+s*2048,r3+s*2048+512);
    SB0();
    UNROLL(4,I,ldp(1,IC<I>{}));
    float c0=0.f,c1=0.f;
    for(int s=0;s<5;s++){ c0=dot2(rf[s].x,C11,c0); c1=dot2(rf[s].y,C11,c1); c0=dot2(rf[s].z,C11,c0); c1=dot2(rf[s].w,C11,c1); }
    float ct=xh_add(c0+c1); unsigned hi=pk2(ct,0.f); rcp_=pk2(ct,ct-blo(hi)); PIN(rcp_);
    for(int s=0;s<5;s++) pinA(rf[s]);
  }
  unsigned kra[2], wb[2][3];
  unsigned C01=0x00003f80u, C10=0x3f800000u; PIN(C01); PIN(C10);
  unsigned kia; { int wq=(tid*1192)>>16, r=tid-wq*55, qu=(r*187)>>11, dp=r-qu*11; kia=sel(tid<220,M_KVI+(((wq>>1)*22+2*dp)*2+(wq&1))*48+qu*8,M_CSX); PIN(kia); }
  { const int h2=(tok>>2)&1, q=(tok&3)+4*(tok>>3);
    for(int a=0;a<2;a++){ unsigned d=sel(q==12,20,21); d=sel(q<12 && !h2,a==0? 8+q:q-8,d); d=sel(q<8,4*a+8*h2+q,d); kra[a]=M_KVI+a*2112+d*96+hh*48; PIN(kra[a]); } }
  { const unsigned rb=M_WO+160*(16*hh+4*blk+q4);
    wb[0][0]=rb+16*p4+8*(p4>>1); wb[0][1]=wb[0][0]^8; wb[0][2]=rb+128+8*p4;
    for(int t=0;t<3;t++){ wb[1][t]=wb[0][t]+61440; PIN(wb[0][t]); PIN(wb[1][t]); } }
  unsigned r4a, r4b, d4;
  { const unsigned r4=M_R4+(w>>1)*5120+(8*g4+q4)*160; r4a=r4+64*(w&1)+16*p4; r4b=r4+128+8*p4; PIN(r4a); PIN(r4b);
    const int js3=(js*43)>>7;
    d4= job? WS_WT4+(ju&7)*286720+(js<48? 16*(js-3*js3)+((js3-(AROT? 2*(ju&7):0))&15) : js)*5120 : WS_END; PINS(d4); }
  c.src3 = WS_WT3 + pj*286720 + w*5120;
  c.src4 = WS_WT4 + cc*286720 + w*5120;
  c.wlo = WS_WT4 + cc*286720 + 240*1024 + kh*5120;
  PINS(c.src3);
  SB0();
  UNROLL(4,I,ldp(1,IC<4+I>{}));
  stamp(ws,c.b,28);
  f32x16 acc[4], acs[4]; v4u xt[2][4]; v4u PW_[4][2]; v2u pf; v4u xr0,xr1; v2u xr2;
  float rstd1, mean1; v4u MB1; f32x4 cv[4]; float cs[4]; v4u cf[4];
  vmwait<(TLBT? 11:10)>(); barrier();
  stamp(ws,c.b,1);
  for(int s=0;s<4;s++) xt[0][s]=*(const LAS v4u*)(xr[s]);
  pipe<200,4>(
    [&](auto i) AI { constexpr int I=i, kc=I/20, r=I%20, t=r&3, o0=(kc%4)*16384; unsigned a=(r<4? aw[t]+o0 : ab[t]+o0+((r-4)/4)*4096); return trfrag(a,a+1024); },
    [&](auto i,v4u A) AI { constexpr int I=i, kc=I/20, r=I%20, t=r&3;
      if constexpr(r<4){ if constexpr(kc==0) mfma32A_0(acs[t],ONE,A); else mfma32A(acs[t],ONE,A); }
      else { if constexpr(kc==0 && r<8) mfma32vv_0(acc[t],A,xt[0][0]); else mfma32vv(acc[t],A,xt[kc&1][(r-4)/4]); } },
    [&](auto i) AI { constexpr int I=i, kc=I/20, r=I%20;
      if constexpr(r%2==0 && r<16 && kc<8) ldp(kc+2,IC<(r/2)&7>{});
      if constexpr(kc==8){
        if constexpr(r==0) asm volatile("buffer_load_dwordx4 %0, %1, %2, %3 offen" : "=v"(xr0) : "v"(xo_), "s"(rx), "s"(t16*20480+cc*160) : "memory");
        if constexpr(r==1) asm volatile("buffer_load_dwordx4 %0, %1, %2, %3 offen offset:64" : "=v"(xr1) : "v"(xo_), "s"(rx), "s"(t16*20480+cc*160) : "memory");
        if constexpr(r==2) asm volatile("buffer_load_dwordx2 %0, %1, %2, %3 offen offset:128" : "=v"(xr2) : "v"(xo2), "s"(rx), "s"(t16*20480+cc*160) : "memory");
        if constexpr(r==5) touch(dummy,ro,wov,((c.b>>3)*200+w*50)*128+(MAPM==8? (cc>>2)*640:0));
        if constexpr(r==7 || r==9 || r==11) rtp(IC<(r-7)/2>{}); }
      if constexpr(r>=17){
        constexpr int n0=bs0(kc), cnt=bs0(kc+1)-n0, lo=n0+(cnt*(r-17))/3, hi=n0+(cnt*(r-16))/3;
        auto bst=[&](auto N) AI { constexpr int n=N;
          if constexpr(n==0) st16(PZ,c.rs,kvo,WS_KVP+(pj*16+pi)*KVP_B);
          if constexpr(n==1) st8(v2u{PZ.x,PZ.y},c.rs,sv,ssb);
          if constexpr(n==2) st16(PZ,c.rs,ov1,osb);
          if constexpr(n==3) st16(PZ,c.rs,ov2,osb);
          if constexpr(n==4) st16(PZ,c.rs,ov1,osb+768);
          if constexpr(n==5) st16(PZ,c.rs,hv2,hsb+4096);
          if constexpr(n==6) st16(PZ,c.rs,hv1,hsb);
          if constexpr(n==7) st4(rcp_,c.rs,csv,dcs);
          if constexpr(n==8) st16(PZ,c.rs,hv1,hsb+2048);
          if constexpr(n>=9) st16a(rf[(n+1)%5],c.rs,c.voff,d3+((n+1)%5)*d3s); };
        UNROLL(hi-lo,J,bst(IC<lo+J>{})); }
      if constexpr(r==3){ for(int s=0;s<4;s++){ v4u v=xt[kc&1][s]; sm[0]=dot2(v.x,C11,sm[0]); sq[0]=dot2(v.x,v.x,sq[0]); sm[1]=dot2(v.y,C11,sm[1]); sq[1]=dot2(v.y,v.y,sq[1]);
          sm[0]=dot2(v.z,C11,sm[0]); sq[0]=dot2(v.z,v.z,sq[0]); sm[1]=dot2(v.w,C11,sm[1]); sq[1]=dot2(v.w,v.w,sq[1]); } PIN(sm[0]); PIN(sm[1]); PIN(sq[0]); PIN(sq[1]); }
      if constexpr(r==16 && kc<9){
        if constexpr(KVST) stamp(ws,c.b,(32+2*kc));
        if constexpr(kc==0) vmwait<8>(); else if constexpr(kc==8) vmwait<7>(); else vmwait<8+bs0(kc)-bs0(kc-1)>();
        barrier();
        if constexpr(KVST) stamp(ws,c.b,(32+2*kc+1));
        if constexpr(kc==1){ asm volatile("" : "+v"(ep),"+v"(dummy)); if constexpr(TLBT) PIN(dum2);
          { u64 t=(((u64)ep.y<<32)|ep.x)+0x9E3779B97F4A7C15ull; c.tag=v2u{(unsigned)t,(unsigned)(t>>32)}; asm volatile("" : "+v"(c.tag)); } }
        if constexpr(kc==8){ if(w==1) st8(c.tag,c.rs,c.flagv,WS_FLAG+FK_A*256*128); } }
      if constexpr(r>=16 && kc<9) xt[(kc+1)&1][r&3]=*(const LAS v4u*)(xr[r&3]+((kc+1)%3)*4096);
      if constexpr(kc>=4 && kc<8 && (r==5||r==7||r==11)){ constexpr int a=kc-4;
        unsigned ho=((a<2? (pj&1):(jq&1))*2+(a&1))*800;
        if constexpr(r==5){ v2u v=trd(pwa+ho), u=trd(pwa+ho+320); PW_[a][0]=v4u{v.x,v.y,u.x,u.y}; }
        if constexpr(r==7){ v2u v=trd(pwb+ho+640); PW_[a][1]=v4u{v.x,v.y,0u,0u}; }
        if constexpr(r==11){ PW_[a][1].x= hh? 0u:PW_[a][1].x; PW_[a][1].y= hh? 0u:PW_[a][1].y; pinV(PW_[a][0]); pinV(PW_[a][1]); } }
      if constexpr(kc==9){
        if constexpr(r==4){ asm volatile("" : "+a"(acs[0]),"+a"(acs[1]),"+a"(acs[2]),"+a"(acs[3])); for(int t=0;t<4;t++) *(LAS float*)(csw+t*512)=acs[t][0]; }
        if constexpr(r==5){ sm[0]+=sm[1]; sq[0]+=sq[1]; PIN(sm[0]); PIN(sq[0]); }
        if constexpr(r==6){ sm[0]=xh_add(sm[0]); sq[0]=xh_add(sq[0]); PIN(sm[0]); PIN(sq[0]); }
        if constexpr(r==7){ mean1=sm[0]*(-1.f/640.f); PIN(mean1); sq[0]=fmaxf(sq[0]*(1.f/640.f)-mean1*mean1,0.f)+1e-5f; PIN(sq[0]); }
        if constexpr(r==8){ lgk0(); barrier(); for(int t=0;t<4;t++) cv[t]=*(const LAS f32x4*)(csr+t*512); }
        if constexpr(r==9){ rstd1=__builtin_amdgcn_rsqf(sq[0]); PIN(rstd1); unsigned hi=pk2(mean1,0.f); MB1.y=hi; PIN(MB1.y); }
        if constexpr(r==10){ MB1.x=pk2(mean1,mean1-blo(MB1.y)); PIN(MB1.x); }
        if constexpr(r==11){ MB1= hh? v4u{0,0,0,0} : v4u{MB1.x,MB1.y,0,0}; pinV(MB1); }
        if constexpr(r==12){ for(int t=0;t<4;t++){ cs[t]=cv[t][0]+cv[t][1]; PIN(cs[t]); } }
        if constexpr(r==13){ for(int t=0;t<4;t++){ cs[t]+=cv[t][2]; PIN(cs[t]); } }
        if constexpr(r==14){ for(int t=0;t<4;t++){ cs[t]+=cv[t][3]; PIN(cs[t]); } }
        if constexpr(r==15){ for(int t=0;t<4;t++){ cf[t].x=pk2(cs[t],cs[t]); PIN(cf[t].x); } }
        if constexpr(r==16){ for(int t=0;t<4;t++){ cf[t].y=cf[t].x<<16; PIN(cf[t].y); } }
        if constexpr(r==17){ for(int t=0;t<4;t++){ cs[t]-=__uint_as_float(cf[t].y); PIN(cs[t]); } }
        if constexpr(r==18){ for(int t=0;t<4;t++){ cf[t].y=pk2(cs[t],0.f); cf[t].z=cf[t].y; cf[t].w=cf[t].y; pinV(cf[t]); } }
      }
    });
  for(int t=0;t<4;t++) mfma32vv(acc[t],cf[t],MB1);
  stamp(ws,c.b,2);
  v4u q0[2],q1[2];
  { const float vs=hh? rstd1:1.f;
    f32x16 pz[2]; v4u k0[2],k1[4];
    UNROLL(4,I,wop(IC<I>{}));
    asm volatile("s_nop 3" : "+v"(acc[0]),"+v"(acc[1]));
    f32x2 m_[4][4];
    for(int a=0;a<2;a++) k0[a]=pack8r(acc[a],0);
    SB0();
    for(int a=0;a<2;a++) for(int e=0;e<4;e++) m_[a][e]=f32x2{acc[a][8+2*e],acc[a][9+2*e]}*(e<2? vs:rstd1);
    SB0();
    for(int a=0;a<2;a++){ k1[a]=v4u{pk2(m_[a][0][0],m_[a][0][1]),pk2(m_[a][1][0],m_[a][1][1]),pk2(m_[a][2][0],m_[a][2][1]),pk2(m_[a][3][0],m_[a][3][1])}; }
    asm volatile("" : "+v"(acc[2]),"+v"(acc[3]));
    for(int a=2;a<4;a++) for(int e=0;e<(a==2?4:2);e++) m_[a][e]=f32x2{acc[a][8+2*e],acc[a][9+2*e]}*(e<2? vs:rstd1);
    for(int a=0;a<2;a++){ pinV(k0[a]); pinV(k1[a]); }
    SB0();
    for(int a=0;a<2;a++){ mfma32vv_0(pz[a],PW_[a][0],k0[a]); mfma32vv(pz[a],PW_[a][1],k1[a]); }
    SB0();
    UNROLL(2,I,wop(IC<4+I>{}));
    k1[2]=v4u{pk2(m_[2][0][0],m_[2][0][1]),pk2(m_[2][1][0],m_[2][1][1]),pk2(m_[2][2][0],m_[2][2][1]),pk2(m_[2][3][0],m_[2][3][1])};
    k1[3]=v4u{pk2(m_[3][0][0],m_[3][0][1]),pk2(m_[3][1][0],m_[3][1][1]),0,0};
    for(int a=0;a<2;a++){ q0[a]=pack8r(acc[2+a],0); q1[a]=k1[2+a]; pinV(q0[a]); pinV(q1[a]); }
    for(int t=0;t<4;t++) *(LAS v2u*)(vwa[t]) = v2u{sel(hh,k1[t].x,0x00003f80u),sel(hh,k1[t].y,0u)};
    *(LAS v2u*)(vwb[0]) = v2u{k1[0].z,k1[0].w}; *(LAS v2u*)(vwb[1]) = v2u{k1[1].z,k1[1].w}; *(LAS v2u*)(vwb[1]+64) = v2u{k1[2].z,k1[2].w};
    SB0(); asm volatile("" : "+v"(pz[0]),"+v"(pz[1]));
    unsigned ph[2][10]; hedgehog2(pz[0],pz[1],hh,rstd1*LOG2E,ph,[&](int k) AI { if(k==0){ wop(IC<6>{}); wop(IC<7>{}); } if(k==2){ wop(IC<8>{}); wop(IC<9>{}); } });
    for(int a=0;a<2;a++) for(int i=0;i<5;i++) *(LAS v2u*)(prow+a*80+i*8)=v2u{ph[a][2*i],ph[a][2*i+1]};
  }
  vmwait<10>(); lgk0(); barrier();
  asm volatile("" : "+v"(xr0),"+v"(xr1),"+v"(xr2),"+v"(dummy));
  stamp(ws,c.b,3);
  f32x16 pq[2];
  v4u r5,r6,r7;
  { v4u fa[8],fb[8];
    #pragma unroll
    for(int s=0;s<3;s++){ fa[s]=trfrag(pa+s*2688,pa+s*2688+672); fb[s]=trfrag(va+s*1664,va+s*1664+416); }
    SB0();
    f32x16 kv;
    #pragma unroll
    for(int s=0;s<8;s++){
      if(s==0) mfma32vv_0(kv,fa[0],fb[0]); else mfma32vv(kv,fa[s],fb[s]);
      SB0();
      if(s+3<8){ fa[s+3]=trfrag(pa+(s+3)*2688,pa+(s+3)*2688+672); fb[s+3]=trfrag(va+(s+3)*1664,va+(s+3)*1664+416); }
      if(s==5) r5=trfrag(r4a,r4a+640);
      if(s==6) r6=trfrag(r4a+8,r4a+648);
      if(s==7) r7=trfrag(r4b,r4b+640);
      SB0(); }
    mfma32vv_0(pq[0],PW_[2][0],q0[0]);
    asm volatile("s_nop 8" : "+v"(kv));
    const unsigned so=WS_KVP+(pj*16+pi)*KVP_B+w*880;
    st8(v2u{pk2(kv[0],kv[1]),pk2(kv[2],kv[3])},c.rs,kvs,so);
    st8(v2u{pk2(kv[4],kv[5]),pk2(kv[6],kv[7])},c.rs,kvs,so+352);
    st8(v2u{pk2(kv[8],kv[9]),pk2(kv[10],kv[11])},c.rs,kvs2,so+704);
    mfma32vv(pq[0],PW_[2][1],q1[0]); mfma32vv_0(pq[1],PW_[3][0],q0[1]); mfma32vv(pq[1],PW_[3][1],q1[1]);
  }
  stamp(ws,c.b,4);
  unsigned phq[2][10];
  ld8(pf,c.rs,pfo,WS_FLAG+FK_A*256*128);
  UNROLL(5,I,wop(IC<10+I>{}));
  {
    barrier();
    stamp(ws,c.b,5);
    asm volatile("s_nop 3" : "+v"(pq[0]),"+v"(pq[1]));
    hedgehog2(pq[0],pq[1],hh,rstd1*LOG2E,phq,[&](int k) AI {
      if(k==0){ st16(r5,c.rs,c.voff,d4+(w&1)*2048); st16(r6,c.rs,c.voff,d4+(w&1)*2048+1024); st16(r7,c.rs,c.voff,(w&1)? d4+4096:WS_END); wop(IC<15>{}); wop(IC<16>{}); }
      if(k==1){ wop(IC<17>{}); wop(IC<18>{}); wop(IC<19>{}); } });
  }
  v4u pb[2][3];
  for(int a=0;a<2;a++){ pb[a][0]=v4u{phq[a][0],phq[a][1],phq[a][2],phq[a][3]}; pb[a][1]=v4u{phq[a][4],phq[a][5],phq[a][6],phq[a][7]}; pb[a][2]=v4u{phq[a][8],phq[a][9],0,0}; for(int s=0;s<3;s++) pinV(pb[a][s]); }
  stamp(ws,c.b,6);
  v2u wf1;
  const unsigned wfo1=(4*min(lane,53)+(cc>>1))*128;
  vmwait<8>(); asm volatile("" : "+v"(pf));
  for(int sp=0;sp<SPIN_CAP;++sp){
    if(!__any(((pf.x^c.tag.x)|(pf.y^c.tag.y))!=0)) break;
    ld8(pf,c.rs,pfo,WS_FLAG+FK_A*256*128); vmwait<0>(); asm volatile("" : "+v"(pf)); }
  barrier();
  stamp(ws,c.b,25);
  v4u WA[12][5];
  { v4u kp[16];
    auto preA=[&](auto i) AI { constexpr int s=i;
      if constexpr(s<12){
        #pragma unroll
        for(int u=0;u<5;u++){ unsigned a=wb[0][u<4? u&1:2]+s*5120+(u<4? 64*(u>>1):0); WA[s][u]=trfrag(a,a+1280); } }
      SB0();
      if constexpr(s>=KL0 && s<KL0+4) UNROLL(4,J,ldV<0>(kp[(s-KL0)*4+J],c.rs,kvo,WS_KVP+(jq*16+(s-KL0)*4+J)*KVP_B));
      if constexpr(s>=8) wop(IC<(12+s)%25>{});
      if constexpr(s>=1){
        #pragma unroll
        for(int u=0;u<5;u++) pinA(WA[s-1][u]); }
      SB0(); };
    UNROLL(13,I,preA(IC<I>{}));
    stamp(ws,c.b,30);
    vmwait<5>(); UNROLL(16,I,pinV(kp[I]));
    for(int sp=0;sp<SPIN_CAP;++sp){
      unsigned m0=0,m1=0; for(int i=0;i<8;i++){ m0=max(m0,max(kp[i].x,kp[i].z)); m1=max(m1,max(kp[8+i].x,kp[8+i].z)); }
      if(!__any(max(m0,m1)==POISON)) break;
      c.nretry+=1<<24;
      UNROLL(16,I,ldV<0>(kp[I],c.rs,kvo,WS_KVP+(jq*16+I)*KVP_B)); vmwait<0>(); UNROLL(16,I,pinV(kp[I]));
    }
    stamp(ws,c.b,23);
    UNROLL(RP3,I,issue_piece<I>(c));
    float s8[8]={};
    for(int i=0;i<16;i++){
      s8[0]=dot2(kp[i].x,C01,s8[0]); s8[1]=dot2(kp[i].x,C10,s8[1]); s8[2]=dot2(kp[i].y,C01,s8[2]); s8[3]=dot2(kp[i].y,C10,s8[3]);
      s8[4]=dot2(kp[i].z,C01,s8[4]); s8[5]=dot2(kp[i].z,C10,s8[5]); s8[6]=dot2(kp[i].w,C01,s8[6]); s8[7]=dot2(kp[i].w,C10,s8[7]); SB0(); }
    *(LAS v2u*)kia=v2u{pk2(s8[0],s8[1]),pk2(s8[2],s8[3])}; *(LAS v2u*)(kia+96)=v2u{pk2(s8[4],s8[5]),pk2(s8[6],s8[7])};
    lgk0(); barrier();
    if(w==2) st8(c.tag,c.rs,c.flagv,WS_FLAG+FK_W*256*128);
  }
  stamp(ws,c.b,24);
  { v4u ka[2][3];
    for(int a=0;a<2;a++) for(int s=0;s<3;s++) ka[a][s]=*(const LAS v4u*)(kra[a]+16*s);
    SB0();
    f32x16 o[2];
    for(int a=0;a<2;a++){ mfma32vv_0(o[a],ka[a][0],pb[a][0]); mfma32vv(o[a],ka[a][1],pb[a][1]); mfma32vv(o[a],ka[a][2],pb[a][2]); }
    mdone(o[1]); asm volatile("" : "+v"(o[0]));
    float r[2]; for(int a=0;a<2;a++) r[a]=__builtin_amdgcn_rcpf(o[a][12]+1e-6f);
    SB0();
    f32x2 y[2][6]; for(int a=0;a<2;a++) for(int e=0;e<6;e++) y[a][e]=f32x2{o[a][2*e],o[a][2*e+1]}*r[a];
    SB0();
    unsigned u_[2][6]; for(int a=0;a<2;a++) for(int e=0;e<6;e++) u_[a][e]=pk2(y[a][e][0],y[a][e][1]);
    st16(v4u{u_[0][0],u_[0][1],u_[0][2],u_[0][3]},c.rs,ov1,osb); st16(v4u{u_[1][0],u_[1][1],u_[1][2],u_[1][3]},c.rs,ov1,osb+768); st16(v4u{u_[0][4],u_[0][5],u_[1][4],u_[1][5]},c.rs,ov2,osb);
  }
  stamp(ws,c.b,7);
  v4u hr0,hr1; v2u hr2;
  { unsigned pr, prv=tk*1280+512+240;
    v4u WB[4][5]; v4u O[20]; const unsigned oo=WS_O+t16*20480;
    auto oload=[&](auto i) AI { constexpr int s=i; int sp=s+orot; sp= sp>=20? sp-20:sp; ldV<0>(O[s],c.rs,c.voff,oo+sp*1024); };
    UNROLL(RP4,I,issue_piece<RP3+I>(c));
    #pragma unroll
    for(int s=0;s<4;s++)
      #pragma unroll
      for(int u=0;u<5;u++){ unsigned a=wb[1][u<4? u&1:2]+s*5120+(u<4? 64*(u>>1):0); WB[s][u]=trfrag(a,a+1280); }
    #pragma unroll
    for(int s=0;s<4;s++)
      #pragma unroll
      for(int u=0;u<5;u++) pinV(WB[s][u]);
    if constexpr(ODLY>0) __builtin_amdgcn_s_sleep(ODLY);
    stamp(ws,c.b,16);
    auto again=[&]() AI {
      for(int s2=0;s2<SPIN_CAP;++s2){
        asm volatile("buffer_load_dword %0, %1, %2, %3 offen sc1\n\ts_waitcnt vmcnt(0)" : "=v"(pr) : "v"(prv), "s"(c.rs), "s"(oo) : "memory");
        if(!__any(pr==POISON)) break;
        c.nretry+=1<<28;
        if constexpr(PSLP>0) __builtin_amdgcn_s_sleep(PSLP); }
      UNROLL(12,J,oload(IC<J>{})); };
    again();
    barrier();
    stamp(ws,c.b,17);
    f32x4 a2[5];
    v4u WC[4][5];
    unsigned omax;
    auto p2step=[&](auto i) AI { constexpr int I=i, s=I/5, u=I%5;
      if constexpr(u==0 && s%2==0) vmwait<(s<=9+RP2? 10:19+RP2-s)>();
      if constexpr(s==0) mfma16a_0(a2[u],WA[0][u],O[0]);
      else if constexpr(s<12) mfma16a(a2[u],WA[s<12?s:0][u],O[s]);
      else if constexpr(s<16) mfma16(a2[u],WB[(s-12)&3][u],O[s]);
      else mfma16(a2[u],WC[(s-16)&3][u],O[s]);
      SB0();
      if constexpr(u==1){ if constexpr(s<8) oload(IC<(12+s)%20>{}); else if constexpr(s==8) ld8(wf1,c.rs,wfo1,WS_FLAG+FK_W*256*128); else if constexpr(s<9+RP2) issue_piece<(s+RP3+RP4-9)>(c); }
      if constexpr(u==3 && s%2==0) asm volatile("v_max3_u32 %0, %0, %1, %2" : "+v"(omax) : "v"(O[s].x), "v"(O[s+1].x));
      if constexpr(I>=20 && I<60 && I%2==0){ constexpr int n=(I-20)/2, s2=n/5, u2=n%5; unsigned a=wb[1][u2<4? u2&1:2]+(4+s2)*5120+(u2<4? 64*(u2>>1):0); WC[s2][u2]=trfrag(a,a+1280); }
      SB0(); };
    for(int sp=0;;++sp){
      omax=0;
      UNROLL(100,I,p2step(IC<I>{}));
      if(!__any(omax==POISON) || sp>=REDO_CAP) break;
      vmwait<0>(); if constexpr((RP3+RP4)%5!=0 && (RP3+RP4)%5!=4) issue_piece<(RP3+RP4)/5*5>(c);
      again(); vmwait<0>(); UNROLL(12,J,pinV(O[J]));
    }
    f32x2 xf[10]; { unsigned xw[10]={xr0.x,xr0.y,xr0.z,xr0.w,xr1.x,xr1.y,xr1.z,xr1.w,xr2.x,xr2.y}; for(int i=0;i<10;i++) xf[i]=f32x2{blo(xw[i]),bhi(xw[i])}; }
    SB0();
    asm volatile("" : "+v"(a2[0]),"+v"(a2[1]),"+v"(a2[2]),"+v"(a2[3]),"+v"(a2[4]));
    stamp(ws,c.b,26);
    const bool sw=g4>=2;
    float hv_[20];
    for(int r=0;r<4;r++){ hv_[r]= sw? a2[1][r]:a2[0][r]; hv_[4+r]= sw? a2[0][r]:a2[1][r]; hv_[8+r]= sw? a2[3][r]:a2[2][r]; hv_[12+r]= sw? a2[2][r]:a2[3][r]; hv_[16+r]=a2[4][r]; }
    SB0();
    for(int i=0;i<10;i++) xf[i]+=f32x2{hv_[2*i],hv_[2*i+1]};
    SB0();
    unsigned hw[10]; for(int i=0;i<10;i++) hw[i]=pk2(xf[i][0],xf[i][1]);
    hr0=v4u{hw[0],hw[1],hw[2],hw[3]}; hr1=v4u{hw[4],hw[5],hw[6],hw[7]}; hr2=v2u{hw[8],hw[9]};
    st16(hr0,c.rs,hv1,hsb); st16(hr1,c.rs,hv1,hsb+2048);
    { auto sx=__builtin_amdgcn_permlane16_swap(hr2.x,hr2.x,false,false); auto sy=__builtin_amdgcn_permlane16_swap(hr2.y,hr2.y,false,false);
      st16(v4u{sx[0],sy[0],sx[1],sy[1]},c.rs,hv2,hsb+4096); }
    float s1=0.f,s1b=0.f; for(int i=0;i<5;i++){ s1=dot2(hw[2*i],C11,s1); s1b=dot2(hw[2*i+1],C11,s1b); }
    for(int i=0;i<10;i++) xf[i]=f32x2{blo(hw[i]),bhi(hw[i])};
    s1=xh_add(x16_add(s1+s1b))*(1.f/80.f);
    SB0();
    for(int i=0;i<10;i++) xf[i]-=s1;
    SB0();
    f32x2 s2={0.f,0.f}, s3={0.f,0.f}; for(int i=0;i<5;i++){ s2=__builtin_elementwise_fma(xf[2*i],xf[2*i],s2); s3=__builtin_elementwise_fma(xf[2*i+1],xf[2*i+1],s3); }
    s2+=s3;
    float s2t=xh_add(x16_add(s2[0]+s2[1]));
    st8(v2u{__float_as_uint(s1),__float_as_uint(s2t)},c.rs,sv,ssb);
    stamp(ws,c.b,18);
  }
  vmwait<4+RP2>(); asm volatile("" : "+v"(wf1));
  UNROLL(NPRE-RP3-RP4-RP2,I,issue_piece<RP3+RP4+RP2+I>(c));
  for(int sp=0;sp<SPIN_CAP;++sp){
    if(!__any(((wf1.x^c.tag.x)|(wf1.y^c.tag.y))!=0)) break;
    c.nretry+=1<<24;
    ld8(wf1,c.rs,wfo1,WS_FLAG+FK_W*256*128); vmwait<0>(); asm volatile("" : "+v"(wf1));
  }
  for(int i=0;i<3;i++){ c.lp[i]=lane*16 + i*65536; c.lq[i]=lane*16 + i*61440 + kh*5120; asm volatile("" : "+v"(c.lp[i]), "+v"(c.lq[i])); }
  c.actst = WS_ACT + ((pi*4+w)*2+blk)*55296 + hh*256 + tk*16; PIN(c.actst);
  c.actld = WS_ACT + (pi*8+rh*4+2*th)*55296 + kh*1024;
  c.av[0]=lane*16+kh*55296; c.av[1]=lane*16+(1-kh)*55296; PIN(c.av[0]); PIN(c.av[1]);
  v2u stt[4];
  c.xo = WS_H + (pi*4+w)*40960; c.rot=cc; c.pollv=(pi*16+tk)*128; PIN(c.pollv);
  unsigned stv=(4*hh)*1024+(32*w+tok)*8; PIN(stv);
  stamp(ws,c.b,19);
  auto window=[&]() AI {
    for(int sp=0;sp<SPIN_CAP;++sp){
      for(int q=0;q<4;q++) ld8(stt[q],c.rs,stv,WS_ST+pi*8192+q*1024);
      vmwait<0>();
      asm volatile("" : "+v"(stt[0]),"+v"(stt[1]),"+v"(stt[2]),"+v"(stt[3]));
      if(!__any(max(max(stt[0].x,stt[1].x),max(stt[2].x,stt[3].x))==POISON)) break;
      c.nretry+=1;
      if constexpr(PSLP>0) __builtin_amdgcn_s_sleep(PSLP);
    }
    UNROLL(XPRE,I,xload<I>(c)); };
  Epi ew;
  for(int sp=0;;++sp){
    window();
    if(sp==0) barrier();
    for(int r=0;r<RA;r++) c.A[r]=ldsA(c,r);
    { float m0=__uint_as_float(stt[0].x),m1=__uint_as_float(stt[1].x),m2=__uint_as_float(stt[2].x),m3=__uint_as_float(stt[3].x);
      float mean=xh_add((m0+m1)+(m2+m3))*0.125f;
      float q=(__uint_as_float(stt[0].y)+__uint_as_float(stt[1].y))+(__uint_as_float(stt[2].y)+__uint_as_float(stt[3].y));
      q += 80.f*(((m0-mean)*(m0-mean)+(m1-mean)*(m1-mean))+((m2-mean)*(m2-mean)+(m3-mean)*(m3-mean)));
      q = xh_add(q);
      c.var=q*(1.f/640.f)+1e-5f; float rs=__builtin_amdgcn_rsqf(c.var); c.c1r=GC1*rs; c.c2r=GC2*rs*rs*rs;
      unsigned mb=pk2(-mean,-mean); c.MB=v4u{mb,mb,mb,mb}; pinV(c.MB); PIN(c.var); PIN(c.c1r); PIN(c.c2r);
    }
    c.xmax=0;
    vmwait<XPRE-1>();
    SB0();
    stamp(ws,c.b,20);
    UNROLL(G1,I,gapx<I>(c,ew));
    if(__builtin_expect(!__any(c.xmax==POISON) || sp>=REDO_CAP,1)) break;
    c.nretry+=1;
    vmwait<0>(); lgk0();
  }
  UNROLL(GN-G1,I,gapx<G1+I>(c,ew));
  stamp(ws,c.b,21);
  { f32x2 hf[10]; { unsigned hw[10]={hr0.x,hr0.y,hr0.z,hr0.w,hr1.x,hr1.y,hr1.z,hr1.w,hr2.x,hr2.y}; for(int i=0;i<10;i++) hf[i]=f32x2{blo(hw[i]),bhi(hw[i])}; }
    SB0();
    asm volatile("" : "+v"(c.acc4[1][0]),"+v"(c.acc4[1][1]),"+v"(c.acc4[1][2]),"+v"(c.acc4[1][3]),"+v"(c.acc4[1][4]));
    for(int u=0;u<5;u++) *(LAS f32x4*)(c.lp[0]+w*5120+u*1024) = c.acc4[1][u];
    asm volatile("" : "+v"(c.acc4[0][0]),"+v"(c.acc4[0][1]),"+v"(c.acc4[0][2]),"+v"(c.acc4[0][3]),"+v"(c.acc4[0][4]));
    for(int i=0;i<10;i++) hf[i]+=f32x2{c.acc4[0][i/2][2*(i&1)],c.acc4[0][i/2][2*(i&1)+1]};
    const v4u rout=mkrs(ar.out,S*D*2);
    lgk0(); barrier();
    f32x4 r[5]; for(int u=0;u<5;u++) r[u]=*(const LAS f32x4*)(c.lp[0]+(w^1)*5120+u*1024);
    unsigned ow[10];
    for(int i=0;i<10;i++){ f32x2 y=hf[i]+f32x2{r[i/2][2*(i&1)],r[i/2][2*(i&1)+1]}; ow[i]=pk2(y[0],y[1]); }
    const unsigned so=t16*20480+cc*160, vo=tk*1280+g4*16;
    asm volatile("buffer_store_dwordx4 %0, %1, %2, %3 offen" :: "v"(v4u{ow[0],ow[1],ow[2],ow[3]}), "v"(vo), "s"(rout), "s"(so) : "memory");
    asm volatile("buffer_store_dwordx4 %0, %1, %2, %3 offen offset:64" :: "v"(v4u{ow[4],ow[5],ow[6],ow[7]}), "v"(vo), "s"(rout), "s"(so) : "memory");
    asm volatile("buffer_store_dwordx2 %0, %1, %2, %3 offen offset:128\n\ts_nop 1" :: "v"(v2u{ow[8],ow[9]}), "v"(vo-g4*8), "s"(rout), "s"(so) : "memory");
  }
  if(lid==0 && w==0) st8(c.tag,c.rs,c.flagv,WS_EPOCH);
  asm volatile(".Lhg_code_end:");
  stamp(ws,c.b,22);
}

extern "C" void kernel_launch(void* const* d_in, const int* in_sizes, int n_in,
                              void* d_out, int out_size, void* d_ws, size_t ws_size,
                              hipStream_t stream) {
  Args a; a.x=(const u16*)d_in[0]; a.wqkv=(const u16*)d_in[1]; a.wphi=(const u16*)d_in[2]; a.wo=(const u16*)d_in[3];
  a.wg=(const u16*)d_in[5]; a.wu=(const u16*)d_in[6]; a.wd=(const u16*)d_in[7]; a.out=(u16*)d_out; a.ws=(char*)d_ws;
  hipFuncSetAttribute((const void*)blk_k, hipFuncAttributeMaxDynamicSharedMemorySize, 163840);
  for(int r=0;r<REPS;r++) blk_k<<<256,256,163840,stream>>>(a);
}
```

```cpp
#define DG1 0
#define RAV 6
#define FFST 0
#define SYL 200
#define PCD 28
#define HOTW 0
#define FS0 160
#define FS1 220
#define PAIRST 0
#define BARL 1
#define FS3 20
#define HOTA 0
#define NOAS 0
#define XPV 12
#define NPRE 30
#define WLD 310
#define TG1 35
#define TG3 38
#define TG4 37
#define TOP 70
#define QAL 280
#define PL0 203
#define PL1 250
#define PL2 6
#define PL3 52
#define XROT 1
#define AROT 0
constexpr int PW = 2;
constexpr int G3 = 280, GE = 288, GN = GE+135, NSTG = 26, RA = RAV, NA = RAV+2;
constexpr int G1 = 40*PW, EP0 = G1+4, EPL = 7, XPRE = XPV, XVER = 0;
enum { OP_NONE=0, OP_XL, OP_DMA, OP_ST, OP_WL, OP_AL, OP_PL, OP_CS };
constexpr bool is_sync(int g){ return g<G3 ? (g%(g<SYL? 40:20)==0 && g>=G1) : (g>=GE && g<GE+120 && (g-GE)%20==0); }
constexpr int sync_stage(int g){ return g<G3 ? g/20 : 14+(g-GE)/10; }
constexpr int sync_need(int g){ int m = g<G3 ? g/20+(g<SYL? 2:1) : 14+(g-GE)/10+2; return m<NSTG? m : NSTG-1; }
constexpr int ep_start(int t){ int g=EP0; for(int i=1;i<=t;i++){ g+=4*EPL; if(i>=PW && g<40*i+44) g=40*i+44; } return g; }
constexpr int PLG[4] = {PL0, PL1, GE+PL2, GE+PL3};
constexpr int PCG[4] = {PL0+PCD, PL1+PCD, GE+PL2+PCD, GE+PL3+PCD};
constexpr int FSG[4] = {FS0, FS1, 260, GE+FS3};
constexpr int FST[4] = {1, 3, 4, 6};
struct Sched { int op[GN+1]; int ix[GN+1]; int cnt[GN+2]; int dmag[NSTG]; int stg[7]; int xg[40]; int ag[54]; int wg[15]; int cg[7]; int ept[GN+1]; };
constexpr int al_kind(int i){ return i<32 ? i/16 : i<40 ? 2 : 3; }
constexpr int cs_use(int t){ return t<PW ? G1-PW+t : 40*t+39; }
constexpr Sched make_sched(){
  Sched s{};
  for(int g=0;g<=GN;g++) s.ept[g]=-1;
  for(int t=0;t<6;t++){ int g0=ep_start(t); for(int e=0;e<4*EPL;e++) s.ept[g0+e]=t*4*EPL+e; int g=g0+4*EPL; if(PAIRST && !(t&1)) g=ep_start(t+1)+4*EPL-1; s.op[g]=OP_ST; s.ix[g]=t; s.stg[t]=g; }
  s.op[GE-1]=OP_ST; s.ix[GE-1]=6; s.stg[6]=GE-1;
  for(int k=0;k<4;k++){ s.op[PLG[k]]=OP_PL; s.ix[PLG[k]]=k; }
  for(int m=0;m<NSTG;m++) s.dmag[m]=-1;
  for(int i=0;i<XPRE;i++) s.xg[i]=-1;
  int ddl[NSTG]={};
  for(int m=0;m<NSTG;m++) for(int q=0;q<GN;q++) if(is_sync(q) && sync_need(q)>=m){ ddl[m]=q; break; }
  int nx=XPRE, nd=NPRE, na=0, nw=0, nc=0, cur=0, now=0, busy=QAL+2*TOP;
  for(int g=0; g<GN; g++){
    if(is_sync(g)) cur=sync_stage(g);
    now += g<EP0 ? TG1 : g<G3 ? TG3 : g<GE ? 40 : TG4;
    if(busy<now) busy=now;
    if(s.op[g]!=OP_NONE){ busy+=34; continue; }
    if(busy-now>QAL) continue;
    int pick=OP_NONE;
    if(nx<40) pick=OP_XL;
    else {
      int dd=1<<20, da=1<<20, dc=1<<20;
      if(nd<NSTG*5 && nd/5<=cur+7) dd=ddl[nd/5];
      if(na<54 && g>=PCG[al_kind(na)]) da=GE+(na/2)*5;
      if(nc<7) dc=cs_use(nc);
      int dw = (nw<15 && g>=60) ? WLD : (1<<20);
      int mn = dd<da? dd:da; mn = mn<dc? mn:dc; mn = mn<dw? mn:dw;
      if(mn<(1<<20)) pick = mn==dc? OP_CS : mn==da? OP_AL : mn==dd? OP_DMA : OP_WL;
    }
    if(pick==OP_NONE) continue;
    s.op[g]=pick; busy+=TOP;
    if(pick==OP_XL){ s.ix[g]=nx; s.xg[nx]=g; nx++; }
    if(pick==OP_DMA){ s.ix[g]=nd; s.dmag[nd/5]=g; nd++; }
    if(pick==OP_AL){ s.ix[g]=na; s.ag[na]=g; na++; }
    if(pick==OP_WL){ s.ix[g]=nw; s.wg[nw]=g; nw++; }
    if(pick==OP_CS){ s.ix[g]=nc; s.cg[nc]=g; nc++; }
  }
  s.cnt[0]=0;
  for(int g=0;g<=GN;g++) s.cnt[g+1]=s.cnt[g]+(s.op[g]!=OP_NONE);
  return s;
}
constexpr Sched SC = make_sched();
constexpr bool sched_ok(){
  if(NPRE%5 || NPRE<25 || NPRE>35) return false;
  for(int g=0;g<GN;g++) if(is_sync(g) && SC.dmag[sync_need(g)]>=g) return false;
  for(int m=NPRE/5;m<NSTG;m++) if(SC.dmag[m]<0) return false;
  for(int s=XPRE;s<40;s++) if(SC.xg[s]>=PW*s-2) return false;
  for(int i=0;i<54;i++) if(SC.ag[i]<=0 || SC.ag[i]>=GE+(i/2)*5-1) return false;
  for(int i=0;i<15;i++) if(SC.wg[i]<=0 || SC.wg[i]>=SC.ag[49]) return false;
  for(int i=0;i<7;i++) if(SC.cg[i]<=0 || SC.cg[i]>=cs_use(i)) return false;
  for(int k=0;k<4;k++) if(SC.stg[FST[k]]>=FSG[k] || !is_sync(FSG[k]) || SC.op[PLG[k]]!=OP_PL) return false;
  for(int t=0;t<7;t++) if(SC.op[SC.stg[t]]!=OP_ST) return false;
  if(SC.stg[5]>=G3) return false;
  return true;
}
static_assert(sched_ok(), "schedule");
constexpr int after(int g, int g0){ return SC.cnt[g]-SC.cnt[g0+1]; }

struct Ctx {
  v4u rs; char* ws; unsigned lp[3], lq[3]; unsigned voff; int lane, w, b, pi, pj; v2u tag;
  unsigned src3, src4, actst, actld, av[2], xo, wlo, pollv, flagv, rot, xmax;
  v4u X[40]; v4u CS[7]; v4u WL[15]; v4u MB; v4u A[NA];
  f32x16 acc[7]; f32x4 acc4[2][5]; v4u B[54]; v2u pf;
  float c1r, c2r, var; float t0,t1,e0,e1,v0,v1; unsigned pk[2][4];
  unsigned nretry; u64 stt, st2; unsigned stv;
};
constexpr int SLOT[8]={5,6,0,1,2,7,3,4};
constexpr int fpos(int f){ return SLOT[(f/20)%8]*20+f%20; }
DI v4u ldsA(const Ctx& c, int a){
  if(a<G3) return *(const LAS v4u*)(c.lp[fpos(a)/64] + (fpos(a)%64)*1024);
  int q=a-G3; int f=fpos(G3 + (q/5)*10 + q%5);
  return *(const LAS v4u*)(c.lq[f/60] + (f%60)*1024);
}
template<int M> DI void issue_piece(Ctx& c){
  constexpr int stg=M/5, p=M%5, pb = p<4 ? 0 : 4;
  unsigned lds = SLOT[stg%8]*20480 + pb*1024 + c.w*5120;
  unsigned so = (HOTW? c.src3 + (stg%3)*20480 : stg<14 ? c.src3 + stg*20480 : c.src4 + (stg-14)*20480) + pb*1024;
  if constexpr(stg==12 || stg==13) so = c.pj>=12 ? WS_END : so;
  if constexpr(p==pb) dma1(c.rs, lds, c.voff, so);
  else asm volatile("buffer_load_dwordx4 %0, %1, %2 offen offset:%3 sc1 lds" :: "v"(c.voff), "s"(c.rs), "s"(so), "n"(p*1024) : "memory");
}
constexpr float GC1 = -2.302208198f, GC2 = -0.1029432397f;
#define PIN(x) asm volatile("" : "+v"(x))
template<int T,int P,int STEP> DI void epi7(Ctx& c){
  constexpr int q=2*P;
  if constexpr(STEP==0){ c.t0=c.acc[T][q]*c.acc[T][q]; PIN(c.t0); c.t1=c.acc[T][q+1]*c.acc[T][q+1]; PIN(c.t1); c.v0=c.acc[T][q]*c.acc[T][q+8]; PIN(c.v0); }
  if constexpr(STEP==1){ c.t0=__builtin_fmaf(c.t0,c.c2r,c.c1r); PIN(c.t0); c.t1=__builtin_fmaf(c.t1,c.c2r,c.c1r); PIN(c.t1); c.v1=c.acc[T][q+1]*c.acc[T][q+9]; PIN(c.v1); }
  if constexpr(STEP==2){ c.t0*=c.acc[T][q]; PIN(c.t0); c.t1*=c.acc[T][q+1]; PIN(c.t1); }
  if constexpr(STEP==3){ c.e0=__builtin_amdgcn_exp2f(c.t0); PIN(c.e0); c.e1=__builtin_amdgcn_exp2f(c.t1); PIN(c.e1); }
  if constexpr(STEP==4){ c.e0=__builtin_fmaf(c.e0,c.var,c.var); PIN(c.e0); c.e1=__builtin_fmaf(c.e1,c.var,c.var); PIN(c.e1); }
  if constexpr(STEP==5){ c.e0=__builtin_amdgcn_rcpf(c.e0); PIN(c.e0); c.e1=__builtin_amdgcn_rcpf(c.e1); PIN(c.e1); }
  if constexpr(STEP==6){ c.v0*=c.e0; PIN(c.v0); c.v1*=c.e1; PIN(c.v1); c.pk[T&1][P]=pk2(c.v0,c.v1); PIN(c.pk[T&1][P]); }
}
struct Epi { f32x2 t[4], v[4]; };
template<int STEP> DI void epiw(Ctx& c, Epi& e){
  for(int i=0;i<4;i++){ const f32x2 a={c.acc[6][2*i],c.acc[6][2*i+1]};
    if constexpr(STEP==0){ e.t[i]=a*a; e.v[i]=a*f32x2{c.acc[6][2*i+8],c.acc[6][2*i+9]}; }
    if constexpr(STEP==1) e.t[i]=__builtin_elementwise_fma(e.t[i],f32x2{c.c2r,c.c2r},f32x2{c.c1r,c.c1r});
    if constexpr(STEP==2) e.t[i]*=a;
    if constexpr(STEP==3) e.t[i]=f32x2{__builtin_amdgcn_exp2f(e.t[i][0]),__builtin_amdgcn_exp2f(e.t[i][1])};
    if constexpr(STEP==4) e.t[i]=__builtin_elementwise_fma(e.t[i],f32x2{c.var,c.var},f32x2{c.var,c.var});
    if constexpr(STEP==5) e.t[i]=f32x2{__builtin_amdgcn_rcpf(e.t[i][0]),__builtin_amdgcn_rcpf(e.t[i][1])};
    if constexpr(STEP==6) e.v[i]*=e.t[i];
    if constexpr(STEP==7) c.pk[0][i]=pk2(e.v[i][0],e.v[i][1]); }
  if constexpr(STEP<7) asm volatile("" : "+v"(e.t[0]),"+v"(e.t[1]),"+v"(e.t[2]),"+v"(e.t[3]),"+v"(e.v[0]),"+v"(e.v[1]),"+v"(e.v[2]),"+v"(e.v[3]));
}
template<int T> DI void act_store(Ctx& c){
  unsigned so = T*8192 + c.pj*512;
  if((T==6 && c.pj>=12) || NOAS) so = WS_END;
  st16(v4u{c.pk[T&1][0],c.pk[T&1][1],c.pk[T&1][2],c.pk[T&1][3]}, c.rs, c.actst, so);
}
template<int ix> DI void xload(Ctx& c){
  if constexpr(XROT){ constexpr int e=ix%5; ldA<(e&3)*1024>(c.X[ix], c.rs, c.voff, c.xo + ((ix/5+c.rot)&7)*5120 + (e&4)*1024); }
  else ldA<(ix%4)*1024>(c.X[ix], c.rs, c.voff, c.xo + (ix/4)*4096); }
template<int ix> DI void aload(Ctx& c){ constexpr int m=ix/2;
  if constexpr(AROT && m<24) ldV<0>(c.B[ix], c.rs, c.av[ix&1], c.actld + (m/8)*16384+((m+c.rot)&7)*2048);
  else if constexpr(HOTA) ldV<(m&1)*2048>(c.B[ix], c.rs, c.voff, c.xo + ((m/2)%8)*4096);
  else ldV<(m&1)*2048>(c.B[ix], c.rs, c.av[ix&1], c.actld + (m/2)*4096); }
template<int g> DI void vmem_op(Ctx& c){
  constexpr int op=SC.op[g], ix=SC.ix[g];
  if constexpr(op==OP_XL) xload<ix>(c);
  if constexpr(op==OP_DMA) issue_piece<ix>(c);
  if constexpr(op==OP_ST) act_store<ix>(c);
  if constexpr(op==OP_WL) ldA<((ix%5)&3)*1024>(c.WL[ix], c.rs, c.voff, c.wlo + (ix/5)*10240 + ((ix%5)&4)*1024);
  if constexpr(op==OP_CS) ldA<(ix%4)*1024>(c.CS[ix], c.rs, c.voff, (ix==6 && c.pj>=12)? WS_END : WS_CS3 + c.pj*8192 + (ix/4)*4096);
  if constexpr(op==OP_AL) aload<ix>(c);
  if constexpr(op==OP_PL) ld8(c.pf, c.rs, c.pollv, WS_FLAG+(FK_D0+ix)*256*128);
}
constexpr unsigned POISON = 0xffffffffu;
DI void useA(v4u a, v4u b, v4u c, v4u d){ asm volatile("" :: "v"(a),"v"(b),"v"(c),"v"(d)); }
template<int g> DI void sync_pt(Ctx& c){
  if constexpr(is_sync(g)){
    constexpr int m=sync_need(g);
    constexpr int n1 = SC.dmag[m]>=0 ? after(g,SC.dmag[m]) : 63;
    constexpr int k = g==FSG[0]?0 : g==FSG[1]?1 : g==FSG[2]?2 : g==FSG[3]?3 : -1;
    constexpr int n2 = k>=0 ? after(g,SC.stg[FST[k<0?0:k]]) : 63;
    if constexpr((n1<n2?n1:n2)<63) vmwait<(n1<n2?n1:n2)>();
    barrier();
    if constexpr(k>=0){ if(c.w==k) st8(c.tag, c.rs, c.flagv, WS_FLAG+(FK_D0+k)*256*128); }
    SB0();
  }
}
template<int g> DI void gapx(Ctx& c, Epi& ew){
  if constexpr(!BARL) sync_pt<g>(c);
  if constexpr(g<G3){
    constexpr int T = g<G1 ? g%PW : g/40, s = g<G1 ? g/PW : g%40;
    if constexpr(g%4==0) useA(c.A[g%NA],c.A[(g+1)%NA],c.A[(g+2)%NA],c.A[(g+3)%NA]);
    if constexpr(s==0) mfma32_0(c.acc[T], c.A[g%NA], c.X[s]); else mfma32(c.acc[T], c.A[g%NA], c.X[s]);
    if constexpr(s==39){ vmwait<after(g,SC.cg[T])>(); mfma32av(c.acc[T], c.CS[T], c.MB); }
    SB0();
    if constexpr(BARL) sync_pt<g>(c);
    c.A[(g+RA)%NA] = ldsA(c, g+RA);
    vmem_op<g>(c);
    if constexpr(g<G1 && s>=XVER && T==0){ pinA(c.X[s]); c.e0=__uint_as_float(c.X[s].x); PIN(c.e0); }
    if constexpr(g<G1 && s>=XVER && T==1){ c.xmax=max(c.xmax,__float_as_uint(c.e0)); PIN(c.xmax); }
    if constexpr(g<G1 && T==PW-1 && s+1>=XVER && s+1<40){ if constexpr(s+1<XPRE) vmwait<XPRE-2-s+SC.cnt[g+1]>(); else vmwait<after(g+1,SC.xg[s+1])>(); }
    if constexpr(SC.ept[g]>=0){ constexpr int e=SC.ept[g]; epi7<e/(4*EPL),(e/EPL)%4,e%EPL>(c); }
  } else if constexpr(g<GE){
    if constexpr(g==G3) asm volatile("s_nop 9" : "+v"(c.acc[6]));
    epiw<g-G3>(c,ew);
    vmem_op<g>(c);
  } else {
    constexpr int q=g-GE, m=q/5, u=q%5, a=G3+q;
    if constexpr(q<120 && q%4==0) useA(c.A[a%NA],c.A[(a+1)%NA],c.A[(a+2)%NA],c.A[(a+3)%NA]);
    if constexpr(q<120){
      if constexpr(m==0) mfma16_0(c.acc4[1][u], c.A[a%NA], c.B[1]); else mfma16(c.acc4[1][u], c.A[a%NA], c.B[2*m+1]);
      SB0();
      if constexpr(BARL) sync_pt<g>(c);
      vmem_op<g>(c);
      SB0();
      if constexpr(m==0) mfma16_0(c.acc4[0][u], c.A[a%NA], c.B[0]); else mfma16(c.acc4[0][u], c.A[a%NA], c.B[2*m]);
    } else { mfma16a(c.acc4[1][u], c.WL[q-120], c.B[2*m+1]); SB0(); vmem_op<g>(c); SB0(); mfma16a(c.acc4[0][u], c.WL[q-120], c.B[2*m]); }
    SB0();
    if constexpr(q+RA<120) c.A[(a+RA)%NA] = ldsA(c, a+RA);
  }
  if constexpr(g>=GE-1 && (g+1-GE)%5==0 && g+1<GN) vmwait<after(g+1,SC.ag[2*((g+1-GE)/5)+1])>();
  { constexpr int k = g+1==PCG[0]?0 : g+1==PCG[1]?1 : g+1==PCG[2]?2 : g+1==PCG[3]?3 : -1;
    if constexpr(k>=0){
      vmwait<after(g+1,PLG[k])>(); asm volatile("" : "+v"(c.pf));
      if(__builtin_expect(__any(((c.pf.x^c.tag.x)|(c.pf.y^c.tag.y))!=0),0)){ c.nretry+=256<<(4*k);
        for(int sp=0;sp<SPIN_CAP;++sp){ ld8(c.pf, c.rs, c.pollv, WS_FLAG+(FK_D0+k)*256*128); vmwait<0>(); asm volatile("" : "+v"(c.pf)); if(!__any(((c.pf.x^c.tag.x)|(c.pf.y^c.tag.y))!=0)) break; } }
    } }
  SB0();
  if constexpr(g==79) stamp(c.ws,c.b,8);
  if constexpr(g==199) stamp(c.ws,c.b,9);
  if constexpr(g==G3-1) stamp(c.ws,c.b,10);
  if constexpr(g==GE-1) stamp(c.ws,c.b,11);
  if constexpr(g==GE+39) stamp(c.ws,c.b,12);
  if constexpr(g==GE+79) stamp(c.ws,c.b,13);
  if constexpr(g==GE+119) stamp(c.ws,c.b,14);
}
#define MAPM 8
#define OL0 10
#define REPS 1
#define RP0 8
#define OROT 1
#define PZC 0
#define C0A 6
#define RP2 10
#define NOCT 0
#define RP3 10
#define RP4 0
#define KL0 0
#define ODLY 0
#define PSLP 2
#define NOST 0
#define TLBT 0
#define QR0 10
#define QB2 80
#define WQE 0
#define QB3 92
#define QSP 3
#define PBL 9
#define RT0 117
#define QS0 29
#define WQ0 135
#define WQS 3
#define WQN 5
#define QW0 50
#define ZROW 0
#define QK0 68
#define QKS 2
#define OPR 0
#define FB1 0
#define ROT 1
constexpr int REDO_CAP=64;
#define BSM 0
constexpr int BS[6][11]={{0,2,4,6,8,10,12,14,14,14,14},{0,0,5,5,9,9,14,14,14,14,14},{0,0,0,7,7,7,14,14,14,14,14},{0,0,0,0,14,14,14,14,14,14,14},{0,0,0,0,0,0,0,14,14,14,14},{0,0,0,0,0,0,7,14,14,14,14}};
constexpr int bs0(int kc){ return BS[BSM][kc<0?0:kc]; }
template<int I> using IC = std::integral_constant<int,I>;
template<int N,int DP,class RD,class MM,class SD> DI void pipe(RD rd, MM mm, SD sd){
  v4u A[DP+1];
  [&]<int... I>(std::integer_sequence<int,I...>) AI { ((A[I]=rd(IC<I>{})), ...); }(std::make_integer_sequence<int,DP>{});
  SB0();
  auto step=[&](auto i) AI { constexpr int I=i; if constexpr(I%2==0 && I+1<N) asm volatile("" :: "v"(A[I%(DP+1)]),"v"(A[(I+1)%(DP+1)])); mm(i,A[I%(DP+1)]); SB0(); sd(i); if constexpr(I+DP<N) A[(I+DP)%(DP+1)]=rd(IC<I+DP>{}); SB0(); };
  [&]<int... I>(std::integer_sequence<int,I...>) AI { (step(IC<I>{}), ...); }(std::make_integer_sequence<int,N>{});
}
DI void mfma32vv_0(f32x16& c, v4u a, v4u x){ asm volatile("v_mfma_f32_32x32x16_bf16 %0, %1, %2, 0" : "=&v"(c) : "v"(a), "v"(x)); }
DI void mfma32vv(f32x16& c, v4u a, v4u x){ asm volatile("v_mfma_f32_32x32x16_bf16 %0, %1, %2, %0" : "+v"(c) : "v"(a), "v"(x)); }
DI void mfma32A_0(f32x16& c, v4u a, v4u x){ asm volatile("v_mfma_f32_32x32x16_bf16 %0, %1, %2, 0" : "=a"(c) : "v"(a), "v"(x)); }
DI void mfma32A(f32x16& c, v4u a, v4u x){ asm volatile("v_mfma_f32_32x32x16_bf16 %0, %1, %2, %0" : "+a"(c) : "v"(a), "v"(x)); }
DI void mfma32av_0(f32x16& c, v4u a, v4u x){ asm volatile("v_mfma_f32_32x32x16_bf16 %0, %1, %2, 0" : "=&v"(c) : "a"(a), "v"(x)); }
DI void mfma16a_0(f32x4& c, v4u a, v4u x){ asm volatile("v_mfma_f32_16x16x32_bf16 %0, %1, %2, 0" : "=&v"(c) : "a"(a), "v"(x)); }
DI void nop2(){ asm volatile("s_nop 1" ::: "memory"); }
DI void mdone(f32x16& c){ asm volatile("s_nop 11" : "+v"(c)); }
#define PINS(x) asm volatile("" : "+s"(x))
DI float max3(float a,float b,float c){ float r; asm("v_max3_f32 %0, %1, %2, %3" : "=v"(r) : "v"(a),"v"(b),"v"(c)); return r; }
DI float min3(float a,float b,float c){ float r; asm("v_min3_f32 %0, %1, %2, %3" : "=v"(r) : "v"(a),"v"(b),"v"(c)); return r; }
DI f32x2 xh(float v){ auto r=__builtin_amdgcn_permlane32_swap(__float_as_uint(v),__float_as_uint(v),false,false); return f32x2{__uint_as_float(r[0]),__uint_as_float(r[1])}; }
template<class H,class M> DI void hedgehog2(f32x16 p, f32x16 r, bool hh, float sc, unsigned (&o)[2][10], H hook, M mid){
  f32x16 z[2]={p,r};
  float a3[2][3], i3[2][3], mx[2], mn[2];
  for(int a=0;a<2;a++){ float x8=z[a][8],x9=z[a][9],xa=z[a][10],xb=z[a][11]; PIN(xa); PIN(xb); z[a][8]=hh? xa:x8; z[a][9]=hh? xb:x9; }
  for(int a=0;a<2;a++) for(int k=0;k<2;k++){ a3[a][k]=max3(z[a][3*k],z[a][3*k+1],z[a][3*k+2]); i3[a][k]=min3(z[a][3*k],z[a][3*k+1],z[a][3*k+2]); }
  SB0(); mid(IC<0>{}); SB0();
  for(int a=0;a<2;a++){ a3[a][2]=max3(z[a][6],z[a][7],z[a][8]); i3[a][2]=min3(z[a][6],z[a][7],z[a][8]); }
  for(int a=0;a<2;a++){ mx[a]=max3(a3[a][0],a3[a][1],z[a][9]); mn[a]=min3(i3[a][0],i3[a][1],z[a][9]); }
  SB0(); mid(IC<1>{}); SB0();
  for(int a=0;a<2;a++){ mx[a]=max3(mx[a],a3[a][2],a3[a][2]); mn[a]=min3(mn[a],i3[a][2],i3[a][2]); }
  SB0(); mid(IC<2>{}); SB0();
  f32x2 sx[2], sn_[2];
  for(int a=0;a<2;a++){ sx[a]=xh(mx[a]); sn_[a]=xh(mn[a]); }
  SB0();
  for(int a=0;a<2;a++){ mx[a]=max3(sx[a][0],sx[a][1],sx[a][1]); mn[a]=min3(sn_[a][0],sn_[a][1],sn_[a][1]); }
  SB0();
  for(int a=0;a<2;a++){ mx[a]*=-sc; mn[a]*=sc; }
  SB0(); hook(0); SB0();
  f32x2 ep[2][5], en[2][5];
  for(int a=0;a<2;a++) for(int i=0;i<5;i++){ f32x2 v={z[a][2*i],z[a][2*i+1]};
      ep[a][i]=__builtin_elementwise_fma(v,f32x2{sc,sc},f32x2{mx[a],mx[a]}); en[a][i]=__builtin_elementwise_fma(v,f32x2{-sc,-sc},f32x2{mn[a],mn[a]}); }
  SB0();
  asm volatile("" : "+v"(ep[0][0]),"+v"(ep[0][1]),"+v"(ep[0][2]),"+v"(ep[0][3]),"+v"(ep[0][4]),"+v"(en[0][0]),"+v"(en[0][1]),"+v"(en[0][2]),"+v"(en[0][3]),"+v"(en[0][4]));
  asm volatile("" : "+v"(ep[1][0]),"+v"(ep[1][1]),"+v"(ep[1][2]),"+v"(ep[1][3]),"+v"(ep[1][4]),"+v"(en[1][0]),"+v"(en[1][1]),"+v"(en[1][2]),"+v"(en[1][3]),"+v"(en[1][4]));
  auto ex=[&](auto J) AI { constexpr int a=J/5, i=J%5;
    mid(IC<3+J>{}); SB0();
    ep[a][i]=f32x2{__builtin_amdgcn_exp2f(ep[a][i][0]),__builtin_amdgcn_exp2f(ep[a][i][1])}; en[a][i]=f32x2{__builtin_amdgcn_exp2f(en[a][i][0]),__builtin_amdgcn_exp2f(en[a][i][1])};
    asm volatile("" : "+v"(ep[a][i]),"+v"(en[a][i]));
    SB0(); if constexpr(i==4){ hook(1+a); SB0(); } };
  UNROLL(10,J,ex(IC<J>{}));
  f32x2 s0[4], s1[4];
  for(int a=0;a<2;a++){ s0[a]=ep[a][0]+ep[a][1]; s1[a]=ep[a][2]+ep[a][3]; s0[2+a]=en[a][0]+en[a][1]; s1[2+a]=en[a][2]+en[a][3]; }
  SB0();
  for(int a=0;a<2;a++){ s0[a]+=ep[a][4]; s0[2+a]+=en[a][4]; }
  SB0();
  for(int k=0;k<4;k++) s0[k]+=s1[k];
  SB0();
  float t[4]; for(int k=0;k<4;k++) t[k]=s0[k][0]+s0[k][1];
  SB0();
  for(int k=0;k<4;k++) s0[k]=xh(t[k]);
  SB0();
  for(int k=0;k<4;k++) t[k]=s0[k][0]+s0[k][1];
  SB0();
  for(int k=0;k<4;k++) t[k]=__builtin_amdgcn_rcpf(t[k]);
  SB0(); hook(3); SB0();
  for(int a=0;a<2;a++) for(int i=0;i<5;i++){ ep[a][i]*=t[a]; en[a][i]*=t[2+a]; }
  SB0();
  for(int a=0;a<2;a++) for(int i=0;i<5;i++){ o[a][i]=pk2(ep[a][i][0],ep[a][i][1]); o[a][5+i]=pk2(en[a][i][0],en[a][i][1]); }
  SB0();
}
DI v4u pack8r(const f32x16& a, int r0){ return v4u{pk2(a[r0],a[r0+1]),pk2(a[r0+2],a[r0+3]),pk2(a[r0+4],a[r0+5]),pk2(a[r0+6],a[r0+7])}; }

struct Args { const u16 *x,*wqkv,*wphi,*wo,*wg,*wu,*wd; u16* out; char* ws; };
constexpr unsigned M_W=0, M_XS=49152, M_DUM=98304, M_WQ=102400, M_RT=24576, M_R4=M_RT+20480, M_PHI=63488, M_VI=M_PHI+128*168, M_WO=0;
constexpr unsigned M_CSX=L_CSX, M_KVI=L_KVI, M_WP=L_KVI+4224;
template<int OFF> DI void dmaN(v4u rs, unsigned voff, unsigned soff){
  asm volatile("buffer_load_dwordx4 %0, %1, %2 offen offset:%3 lds" :: "v"(voff), "s"(rs), "s"(soff), "n"(OFF) : "memory");
}
DI void dma0nt(v4u rs, unsigned lds, unsigned voff, unsigned soff){
  asm volatile("s_mov_b32 m0, %0\n\ts_nop 0\n\tbuffer_load_dwordx4 %1, %2, %3 offen nt lds" :: "s"(lds), "v"(voff), "s"(rs), "s"(soff) : "memory");
}
template<int OFF> DI void dmaNnt(v4u rs, unsigned voff, unsigned soff){
  asm volatile("buffer_load_dwordx4 %0, %1, %2 offen offset:%3 nt lds" :: "v"(voff), "s"(rs), "s"(soff), "n"(OFF) : "memory");
}
DI void st16a(v4u d, v4u rs, unsigned voff, unsigned soff){
  asm volatile("buffer_store_dwordx4 %0, %1, %2, %3 offen sc1" :: "a"(d), "v"(voff), "s"(rs), "s"(soff) : "memory");
}
template<int OFF> DI void ldsrdA(v4u& d, unsigned a){ asm volatile("ds_read_b128 %0, %1 offset:%2" : "=a"(d) : "v"(a), "n"(OFF) : "memory"); }
template<int OFF> DI void ldAnt(v4u& d, v4u rs, unsigned voff, unsigned soff){
  asm volatile("buffer_load_dwordx4 %0, %1, %2, %3 offen offset:%4 nt" : "=a"(d) : "v"(voff), "s"(rs), "s"(soff), "n"(OFF) : "memory");
}
template<int OFF> DI void ldswrA(unsigned a, v4u d){ asm volatile("ds_write_b128 %0, %1 offset:%2" :: "v"(a), "a"(d), "n"(OFF) : "memory"); }
DI void umax3(unsigned& m, unsigned a, unsigned b){ asm volatile("v_max3_u32 %0, %0, %1, %2" : "+v"(m) : "v"(a), "v"(b)); }
DI unsigned sel(bool c, unsigned a, unsigned b){ return c? a:b; }
DI void touch(unsigned& d, v4u rs, unsigned voff, unsigned soff){
  asm volatile("buffer_load_dword %0, %1, %2, %3 offen" : "=v"(d) : "v"(voff), "s"(rs), "s"(soff) : "memory");
}

__global__ __launch_bounds__(256,1) void blk_k(Args ar){
  extern __shared__ __attribute__((aligned(16))) char smem[];
  char* ws=ar.ws;
  Ctx c;
  c.ws=ws; c.b=blockIdx.x; c.lane=threadIdx.x&63; c.w=__builtin_amdgcn_readfirstlane(threadIdx.x>>6);
  c.pi=((c.b&7)>>2)*8+((c.b>>3)&7); c.pj=(c.b&3)*4+(c.b>>6);
  const int lane=c.lane, w=c.w, pi=c.pi, pj=c.pj, jq=pj^1, tid=threadIdx.x;
  c.stt=0; c.stv=0;
  stamp(ws,c.b,0);
  unsigned dummy;
  { u64 pc; unsigned len; asm volatile("s_getpc_b64 %0\n.Lhg_code_begin:\n\ts_mov_b32 %1, .Lhg_code_end-.Lhg_code_begin" : "=s"(pc), "=s"(len));
    touch(dummy, mkrs((const void*)pc,NOCT? 0:len), sel(lane<4,lane*128,OOB), ((c.b>>3)*16+w*4)*128); }
  const int lid=pi*16+pj; const bool job = lid<216;
  const int jp=(lid*152)>>12, jl=lid-jp*27, ju=lid*2+(w>>1), js=ju>>3;
  const v4u rx=mkrs(ar.x,S*D*2), rw=mkrs(ar.wqkv,D*1920*2);
  const int rot= ROT==1? (3*pi+pj)%10 : (pi+3*pj)%10; int kph=rot;
  const unsigned xs=M_XS+w*12288, xrow=(pi*128+w*32)*1280, wrow=w*16*3840+pj*80;
  const int g4=lane>>4, tk=lane&15;
  unsigned xv[4]; for(int e=0;e<4;e++) xv[e]=(lane>>3)*1280+(((lane&7)^(g4+4*(e&1))))*16+e*(10240-1024);
  unsigned wv[3];
  unsigned xso=xrow+kph*128, wso=wrow+kph*(64*3840);
  auto ldp=[&](int kc,auto P) AI { constexpr int p=P;
    if constexpr(p==0) dma0(rx, xs+(kc%3)*4096, xv[0], xso); else if constexpr(p<4) dmaN<p*1024>(rx,xv[p&3],xso);
    else if constexpr(p==4) dma0(rw, M_W+(kc%4)*12288+w*3072, wv[0], wso); else dmaN<(p-4)*1024>(rw,wv[(p-4)%3],wso);
    if constexpr(p==6){ kph = kph==9? 0:kph+1; xso=xrow+kph*128; wso=wrow+kph*(64*3840); } };
  UNROLL(4,I,ldp(0,IC<I>{}));
  for(int p=0;p<3;p++){ const unsigned idx=p*64+lane, n=(idx*171)>>11, ch=idx-n*12; unsigned cb=sel(ch<5,1280,2480); cb=sel(ch<10,cb,OOB); wv[p]=n*(3840-192)+lane*16+cb; }
  UNROLL(3,I,ldp(0,IC<4+I>{}));
  SB0();
  c.rs=mkrs(ws,WS_END); c.voff=lane*16; c.nretry=0;
  const int tok=lane&31, hh=lane>>5;
  const int cc=pj&7, rh=pj>>3, th=w>>1, kh=w&1, t16=pi*8+rh*4+w;
  const int blk=g4&1, q4=tk>>2, p4=lane&3, qd=blk*4+p4;
  const bool light=WQE? (0xA5>>cc)&1 : false;
  v4u PZ={POISON,POISON,POISON,POISON}; pinV(PZ);
  const unsigned osb=WS_O+(pi*4+w)*40960+jq*1280, hsb=WS_H+(t16>>1)*40960+cc*5120+(t16&1)*256, ssb=WS_ST+pi*8192+cc*1024+(rh*64+w*16)*8;
  unsigned ov=blk*20480+tk*16, ov1=ov+hh*256, ov2=sel(hh,OOB,ov+512), kvo=sel(lane<55,w*896+lane*16,OOB);
  unsigned hv1=tk*16+g4*512, hv2=sel(blk,OOB,tk*16+hh*512), sv=sel(lane<16,tk*8,OOB);
  unsigned pfo=tk*2048, xo_=tk*1280+g4*16, xo2=xo_-g4*8;
  c.flagv=sel(lane==0,lid*128,OOB); PIN(c.flagv);
  PIN(ov1); PIN(ov2); PIN(kvo); PIN(hv1); PIN(hv2); PIN(sv); PIN(pfo); PIN(xo_); PIN(xo2);
  float sm[2]={0.f,0.f}, sq[2]={0.f,0.f};
  unsigned C11=0x3f803f80u; PIN(C11);
  unsigned xr[4]; { unsigned b0=xs+tok*128, x0=(hh^((tok>>1)&7))*16; for(int s=0;s<4;s++){ xr[s]=b0+(x0^(32*s)); PIN(xr[s]); } }
  v4u ONE; { unsigned o1=sel(tok==0,0x3f803f80u,0u); ONE=v4u{o1,o1,o1,o1}; pinV(ONE); }
  unsigned ab[3], aw[3];
  { const unsigned rb=M_W+192*(8*hh+q4);
    ab[0]=rb+8*(qd+sel(qd<5,0,8)); ab[1]=rb+8*(qd+5); ab[2]=rb+8*(16+(ZROW? qd:p4));
    for(int t=0;t<3;t++){ aw[t]=ab[t]+w*3072; PIN(ab[t]); PIN(aw[t]); } }
  const unsigned qd4=8*min(qd,4);
  unsigned pwb=M_WP+q4*40+qd4, pwa=pwb+hh*160; PIN(pwa); PIN(pwb);
  unsigned csw=sel(hh,M_DUM,M_CSX)+lane*16+w*4, csr=M_CSX+tok*16; PIN(csw); PIN(csr);
  unsigned prow=M_PHI+(w*32+tok)*168+hh*40, vrow=M_VI+(w*32+tok)*104; PIN(prow);
  unsigned vwa[2], vwb[2];
  vwa[0]=vrow+40-16*hh; vwa[1]=vrow+88-88*hh; vwb[0]=vrow+32+16*hh; vwb[1]=vrow+8+8*hh;
  for(int t=0;t<2;t++){ PIN(vwa[t]); PIN(vwb[t]); }
  unsigned pa=M_PHI+(8*hh+q4)*168+w*40+qd4, va=M_VI+(8*hh+q4)*104+(w>>1)*48+8*min(qd,5), kvs=sel(tok<22,tok*8+hh*176,OOB), kvs2=sel(hh,OOB,kvs);
  PIN(pa); PIN(va); PIN(kvs); PIN(kvs2);
  *(LAS v2u*)(sel(tid<88,M_KVI+tid*48+40,M_DUM))=v2u{0,0};
  unsigned qv[5], ovv[5], rdv[3];
  for(int p=0;p<5;p++){ int idx=p*64+lane, n=(idx*205)>>10; int row=(n&~15)+4*(n&3)+((n>>2)&3); qv[p]=row*3840-n*80+lane*16+(p&4)*1024; PIN(qv[p]); }
  auto wqp=[&](auto N) AI { constexpr int n=N, p=n%5; const int grp=(n/5)*4+w; int gp=grp+rot; gp= gp>=10? gp-10:gp; const unsigned so=gp*(64*3840)+jq*80;
    if(n<10 || w<2){ if constexpr(p%4==0) dma0(rw,M_WQ+grp*5120+p*1024,qv[p],so); else { dma0(rw,M_WQ+grp*5120+p*1024,qv[p],so+p*1024); } } };
  SB0();
  stamp(ws,c.b,27);
  v2u ep; ld8(ep,c.rs,0u,WS_EPOCH);
  if(lane<50) dma0(mkrs(ar.wphi,NH*400*2), M_WP+w*800, c.voff, (pj>>1)*3200+w*800);
  UNROLL(4,I,ldp(1,IC<I>{}));
  for(int p=0;p<5;p++){ int idx=p*64+lane, n=(idx*205)>>11; int row=(n&~12)|((n&4)<<1)|((n&8)>>1); ovv[p]=row*1280-n*160+lane*16+(p&4)*1024; PIN(ovv[p]);
    if(p<3){ rdv[p]=n*(1280-160)+lane*16; if(p==2) rdv[p]=sel(lane<32,rdv[p],OOB); PIN(rdv[p]); } }
  v4u ro=mkrs(ar.wo,D*D*2), rd=mkrs(ar.wd,job? D*F*2:0), rg=mkrs(w&1? ar.wu:ar.wg,job? D*F*2:0); PINS(ro); PINS(rd); PINS(rg);
  unsigned s3o=(jp*80+(w>>1)*40)*3456+jl*128, s4o=(js*32+(w&1)*16)*1280+(ju&7)*160, rtv=(lane>>3)*3456+(lane&7)*16;
  PINS(s3o); PINS(s4o); PIN(rtv);
  v4u RG[8];
  auto rtp=[&](auto P) AI { constexpr int p=P;
    if constexpr(p<5) ldAnt<0>(RG[p],rg,rtv,s3o+p*8*3456); else ldAnt<(p-5)*1024>(RG[p],rd,rdv[(p-5)%3],s4o); };
  unsigned rw3=M_RT+(w&1)*10240+(w>>1)*5120+lane*16, rw4=M_R4+w*3072+lane*16; PIN(rw3); PIN(rw4);
  unsigned pbo=(pi*16+tk)*128, pco=((lane&7)*27+4*min(lane>>3,pj<12? 6:5)+(pj>>2))*128; PIN(pbo); PIN(pco);
  unsigned wov= MAPM==8? sel(lane<25,((lane*52)>>8)*640+lane*128,OOB) : sel(lane<50,lane*128,OOB); PIN(wov);
  const int orot=OROT? (5*cc)>>1 : 0;
  auto wop=[&](auto N) AI { constexpr int n=N, p=n%5; constexpr int rn=n/5; int s=4*rn+w, sp=s+orot; sp= sp>=20? sp-20:sp; unsigned so=sp*32*1280+cc*160;
    if constexpr(p%4==0) dma0(ro,M_WO+s*5120+p*1024,ovv[p],so); else dmaN<p*1024>(ro,ovv[p],so); };
  unsigned d3, d3s, dcs, d4, r3, r4a, r4b, csv=sel(hh,OOB,tok*16); PIN(csv);
  { r3=M_RT+blk*10240+(8*hh+q4)*128+8*(((p4&1)<<1)|(p4>>1))+w*32;
    const unsigned r4=M_R4+(w>>1)*6144+hh*3072+(8*blk+q4)*160; r4a=r4+64*(w&1)+16*p4; r4b=r4+128+8*p4; PIN(r3); PIN(r4a); PIN(r4b);
    const int tau=jl*4+w, j2=tau&15, i2=tau>>4;
    const int sl0=(jp-(XROT? (j2&7):0))&7;
    d3= job? WS_WT3+j2*286720+(i2<PW? PW*sl0*5+i2 : 40*i2+sl0*5)*1024 : WS_END; d3s= i2<PW? PW*1024:1024;
    dcs=job? WS_CS3+j2*8192+i2*1024+(jp>>2)*512+(jp&3)*4:WS_END;
    d4= job? WS_WT4+(ju&7)*286720+js*5120 : WS_END;
    PINS(d3); PINS(d3s); PINS(dcs); PINS(d4); }
  unsigned qb[2], qw[2]; for(int a=0;a<2;a++){ qb[a]=M_WQ+80*(4*q4+2*hh)+40*a+qd4; if(ZROW>1) qb[a]=sel(qd<5,qb[a],0x40000u); qw[a]=qb[a]+w*12800; PIN(qb[a]); PIN(qw[a]); }
  c.src3 = WS_WT3 + pj*286720 + w*5120;
  c.src4 = WS_WT4 + cc*286720 + w*5120;
  c.wlo = WS_WT4 + cc*286720 + 240*1024 + kh*5120;
  SB0();
  UNROLL(3,I,ldp(1,IC<4+I>{}));
  st16(PZ,c.rs,kvo,WS_KVP+(pj*16+pi)*KVP_B);
  stamp(ws,c.b,28);
  f32x16 acc[3], acs[3]; v4u xt[4]; v4u PW_[4][2]; v2u pf, pb2, pc2; v4u xr0,xr1; v2u xr2;
  float rstd1, mean1; v4u MB1; f32x4 cv[3]; float cs[3]; v4u cf[3];
  auto xrd=[&](auto KC,auto SS) AI { constexpr int kc=KC, s=SS; ldsrdA<(kc%3)*4096>(c.X[kc*4+s],xr[s]); xt[s]=*(const LAS v4u*)(xr[s]+(kc%3)*4096); };
  auto csstep=[&](auto NT_,auto K) AI { constexpr int NT=NT_, k=K;
    if constexpr(k==0){ for(int t=0;t<NT;t++) cv[t]=*(const LAS f32x4*)(csr+t*512); }
    if constexpr(k==2){ for(int t=0;t<NT;t++){ cs[t]=cv[t][0]+cv[t][1]; PIN(cs[t]); cv[t][2]+=cv[t][3]; PIN(cv[t][2]); } }
    if constexpr(k==3){ for(int t=0;t<NT;t++){ cs[t]+=cv[t][2]; PIN(cs[t]); cf[t].x=pk2(cs[t],cs[t]); PIN(cf[t].x); } }
    if constexpr(k==4){ for(int t=0;t<NT;t++){ cf[t].y=cf[t].x<<16; PIN(cf[t].y); cs[t]-=__uint_as_float(cf[t].y); PIN(cs[t]); } }
    if constexpr(k==5){ for(int t=0;t<NT;t++){ cf[t].y=pk2(cs[t],0.f); pinV(cf[t]); } } };
  for(int t=0;t<3;t++){ cf[t]=v4u{0,0,0,0}; pinV(cf[t]); }
  vmwait<10>(); barrier();
  stamp(ws,c.b,1);
  UNROLL(4,I,xrd(IC<0>{},IC<I>{}));
  pipe<150,4>(
    [&](auto i) AI { constexpr int I=i, kc=I/15, r=I%15, t=r%3, o0=(kc%4)*12288; unsigned a=(r<3? aw[t]+o0 : ab[t]+o0+((r-3)/3)*3072); return trfrag(a,a+768); },
    [&](auto i,v4u A) AI { constexpr int I=i, kc=I/15, r=I%15, t=r%3;
      if constexpr(r<3){ if constexpr(kc==0) mfma32A_0(acs[t],ONE,A); else mfma32A(acs[t],ONE,A); }
      else { if constexpr(kc==0 && r<6) mfma32_0(acc[t],A,c.X[0]); else mfma32(acc[t],A,c.X[kc*4+(r-3)/3]); } },
    [&](auto i) AI { constexpr int I=i, kc=I/15, r=I%15;
      constexpr int slot = r==0?0 : r==1?1 : r==3?2 : r==5?3 : r==6?4 : r==8?5 : r==10?6 : -1;
      if constexpr(kc<8 && slot>=0) ldp(kc+2,IC<(slot<0?0:slot)>{});
      if constexpr(I>=RT0 && I<RT0+8) rtp(IC<(I-RT0)&7>{});
      if constexpr(I==RT0+8) touch(dummy,ro,wov,((c.b>>3)*200+w*50)*128+(MAPM==8? (cc>>2)*640:0));
      if constexpr(I>=WQ0 && (I-WQ0)%WQS==0 && (I-WQ0)/WQS<WQN) wqp(IC<((I-WQ0)/WQS)%15>{});
      if constexpr(r==2){ for(int s=0;s<4;s++){ v4u v=xt[s]; sm[0]=dot2(v.x,C11,sm[0]); sq[0]=dot2(v.x,v.x,sq[0]); sm[1]=dot2(v.y,C11,sm[1]); sq[1]=dot2(v.y,v.y,sq[1]);
          sm[0]=dot2(v.z,C11,sm[0]); sq[0]=dot2(v.z,v.z,sq[0]); sm[1]=dot2(v.w,C11,sm[1]); sq[1]=dot2(v.w,v.w,sq[1]); } PIN(sm[0]); PIN(sm[1]); PIN(sq[0]); PIN(sq[1]); }
      if constexpr(r==11 && kc<9){
        if constexpr(KVST) stamp(ws,c.b,(32+2*kc));
        if constexpr(kc==0) vmwait<8>(); else if constexpr(kc==8) vmwait<9>(); else vmwait<7>();
        barrier();
        if constexpr(KVST) stamp(ws,c.b,(32+2*kc+1));
        if constexpr(kc==1){ asm volatile("" : "+v"(ep),"+v"(dummy));
          { u64 t=(((u64)ep.y<<32)|ep.x)+0x9E3779B97F4A7C15ull; c.tag=v2u{(unsigned)t,(unsigned)(t>>32)}; asm volatile("" : "+v"(c.tag)); }
          if(w==1) st8(c.tag,c.rs,c.flagv,WS_FLAG+FK_A*256*128); } }
      if constexpr(r>=11 && kc<9) xrd(IC<(kc+1)%10>{},IC<(r+1)%4>{});
      if constexpr(kc>=4 && kc<8 && (r==4||r==7||r==9)){ constexpr int a=kc-4;
        unsigned ho=((a<2? (pj&1):(jq&1))*2+(a&1))*800;
        if constexpr(r==4){ v2u v=trd(pwa+ho), u=trd(pwa+ho+320); PW_[a][0]=v4u{v.x,v.y,u.x,u.y}; }
        if constexpr(r==7){ v2u v=trd(pwb+ho+640); PW_[a][1]=v4u{v.x,v.y,0u,0u}; }
        if constexpr(r==9){ PW_[a][1].x= hh? 0u:PW_[a][1].x; PW_[a][1].y= hh? 0u:PW_[a][1].y; pinV(PW_[a][0]); pinV(PW_[a][1]); } }
      if constexpr(kc==9){
        if constexpr(r==3){ asm volatile("" : "+a"(acs[0]),"+a"(acs[1]),"+a"(acs[2])); for(int t=0;t<3;t++) *(LAS float*)(csw+t*512)=acs[t][0]; }
        if constexpr(r==4){ sm[0]+=sm[1]; sq[0]+=sq[1]; PIN(sm[0]); PIN(sq[0]); }
        if constexpr(r==5){ sm[0]=xh_add(sm[0]); sq[0]=xh_add(sq[0]); PIN(sm[0]); PIN(sq[0]); }
        if constexpr(r==6){ mean1=sm[0]*(-1.f/640.f); PIN(mean1); sq[0]=fmaxf(sq[0]*(1.f/640.f)-mean1*mean1,0.f)+1e-5f; PIN(sq[0]); }
        if constexpr(r==7){ lgk0(); barrier(); }
        if constexpr(r==8){ rstd1=__builtin_amdgcn_rsqf(sq[0]); PIN(rstd1); MB1.y=pk2(mean1,0.f); PIN(MB1.y); MB1.x=pk2(mean1,mean1-blo(MB1.y)); PIN(MB1.x); }
        if constexpr(r==13){ MB1= hh? v4u{0,0,0,0} : v4u{MB1.x,MB1.y,0,0}; pinV(MB1); }
        if constexpr(r>=7 && r<13) csstep(IC<3>{},IC<(r+5)%6>{});
      }
    });
  for(int t=0;t<3;t++) mfma32vv(acc[t],cf[t],MB1);
  stamp(ws,c.b,2);
  { const float vs=hh? rstd1:1.f;
    f32x16 pz[2]; v4u k0[2],k1[2];
    UNROLL(7-WQN,I,wqp(IC<WQN+I>{}));
    asm volatile("s_nop 7" : "+v"(acc[0]),"+v"(acc[1]));
    f32x2 m_[3][4];
    for(int a=0;a<2;a++) k0[a]=pack8r(acc[a],0);
    SB0();
    for(int a=0;a<2;a++) for(int e=0;e<4;e++) m_[a][e]=f32x2{acc[a][8+2*e],acc[a][9+2*e]}*(e<2? vs:rstd1);
    SB0();
    for(int a=0;a<2;a++){ k1[a]=v4u{pk2(m_[a][0][0],m_[a][0][1]),pk2(m_[a][1][0],m_[a][1][1]),pk2(m_[a][2][0],m_[a][2][1]),pk2(m_[a][3][0],m_[a][3][1])}; }
    asm volatile("" : "+v"(acc[2]));
    for(int e=0;e<4;e++) m_[2][e]=f32x2{acc[2][2*e],acc[2][2*e+1]}*rstd1;
    for(int a=0;a<2;a++){ pinV(k0[a]); pinV(k1[a]); }
    SB0();
    for(int a=0;a<2;a++){ mfma32vv_0(pz[a],PW_[a][0],k0[a]); mfma32vv(pz[a],PW_[a][1],k1[a]); }
    SB0();
    UNROLL(3,I,wqp(IC<7+I>{}));
    for(int t=0;t<2;t++){ *(LAS v2u*)(vwa[t]) = v2u{sel(hh,k1[t].x,0x00003f80u),sel(hh,k1[t].y,0u)}; *(LAS v2u*)(vwb[t]) = v2u{k1[t].z,k1[t].w}; }
    *(LAS v2u*)(vwb[1]+48) = v2u{pk2(m_[2][0][0],m_[2][0][1]),pk2(m_[2][1][0],m_[2][1][1])}; *(LAS v2u*)(vwb[1]+64) = v2u{pk2(m_[2][2][0],m_[2][2][1]),pk2(m_[2][3][0],m_[2][3][1])};
    SB0(); asm volatile("" : "+v"(pz[0]),"+v"(pz[1]));
    unsigned ph[2][10]; hedgehog2(pz[0],pz[1],hh,rstd1*LOG2E,ph,[&](int k) AI {
      if(k==0){ UNROLL(3,I,wqp(IC<10+I>{})); }
      if(k==1){ UNROLL(2,I,wqp(IC<13+I>{})); }
      if(k==2){ vmwait<11>(); UNROLL(5,J,ldswrA<J*1024>(rw3,RG[J])); }
      if(k==3){ UNROLL(3,J,ldswrA<J*1024>(rw4,RG[5+J]));
        asm volatile("buffer_load_dwordx4 %0, %1, %2, %3 offen" : "=v"(xr0) : "v"(xo_), "s"(rx), "s"(t16*20480+cc*160) : "memory");
        asm volatile("buffer_load_dwordx4 %0, %1, %2, %3 offen offset:64" : "=v"(xr1) : "v"(xo_), "s"(rx), "s"(t16*20480+cc*160) : "memory");
        asm volatile("buffer_load_dwordx2 %0, %1, %2, %3 offen offset:128" : "=v"(xr2) : "v"(xo2), "s"(rx), "s"(t16*20480+cc*160) : "memory"); } }, [&](auto) AI {});
    for(int a=0;a<2;a++) for(int i=0;i<5;i++) *(LAS v2u*)(prow+a*80+i*8)=v2u{ph[a][2*i],ph[a][2*i+1]};
  }
  vmwait<3>(); lgk0(); barrier();
  PIN(dummy);
  stamp(ws,c.b,3);
  { v4u fa[8],fb[8];
    #pragma unroll
    for(int s=0;s<3;s++){ fa[s]=trfrag(pa+s*2688,pa+s*2688+672); fb[s]=trfrag(va+s*1664,va+s*1664+416); }
    SB0();
    f32x16 kv;
    #pragma unroll
    for(int s=0;s<8;s++){
      if(s==0) mfma32vv_0(kv,fa[0],fb[0]); else mfma32vv(kv,fa[s],fb[s]);
      SB0();
      if(s+3<8){ fa[s+3]=trfrag(pa+(s+3)*2688,pa+(s+3)*2688+672); fb[s+3]=trfrag(va+(s+3)*1664,va+(s+3)*1664+416); }
      SB0(); }
    mdone(kv);
    const unsigned so=WS_KVP+(pj*16+pi)*KVP_B+w*896;
    st8(v2u{pk2(kv[0],kv[1]),pk2(kv[2],kv[3])},c.rs,kvs,so);
    st8(v2u{pk2(kv[4],kv[5]),pk2(kv[6],kv[7])},c.rs,kvs,so+352);
    st8(v2u{pk2(kv[8],kv[9]),pk2(kv[10],kv[11])},c.rs,kvs2,so+704);
  }
  stamp(ws,c.b,4);
  unsigned phq[2][10]; v4u pb[2][3]; v2u wf1; unsigned kra[2], wb[2][3], kia[2];
  const unsigned wfo1=(4*min(lane,53)+(cc>>1))*128;
  { f32x16 aq[2], rcs; v4u rf[8]; v4u kp[16]; v4u IDN;
    constexpr int B2=QB2, SP=QSP, W1=QW0, K0=QK0, KS=QKS;
    static_assert(1+15*SP<W1 && W1+12<B2 && B2+7<100 && K0>W1+13 && K0+15*KS<100 && B2<=K0+16*KS && 1+7*SP>=14, "Q pass schedule");
    pipe<100,4>(
      [&](auto i) AI { constexpr int I=i; unsigned a=(I<20? qw[I&1]:qb[I&1])+(I<20? I/2:(I-20)/2)*1280; return trfrag(a,a+80); },
      [&](auto i,v4u A) AI { constexpr int I=i;
        if constexpr(I<20){ if constexpr(I<2) mfma32A_0(acs[I&1],ONE,A); else mfma32A(acs[I&1],ONE,A); }
        else { if constexpr(I<22) mfma32_0(aq[I&1],A,c.X[0]); else mfma32(aq[I&1],A,c.X[(I-20)/2]); } },
      [&](auto i) AI { constexpr int I=i;
        if constexpr(I==0) ld8(pf,c.rs,pfo,WS_FLAG+(FK_A*256+jq)*128);
        if constexpr(I<5) rf[I&7]=trfrag(r3+I*2048,r3+I*2048+512);
        if constexpr(I==5) rf[5]=trfrag(r4a,r4a+640);
        if constexpr(I==6) rf[6]=trfrag(r4a+8,r4a+648);
        if constexpr(I==7) rf[7]=trfrag(r4b,r4b+640);
        if constexpr(I>=5 && I<10){ if constexpr(I==5) mfma32vv_0(rcs,ONE,rf[0]); else mfma32vv(rcs,ONE,rf[(I-5)&7]); }
        if constexpr(I>=1 && (I-1)%SP==0 && (I-1)/SP<16){ constexpr int n=(I-1)/SP;
          if constexpr(n==0) st16(PZ,c.rs,ov1,osb);
          if constexpr(n==1) st16(PZ,c.rs,ov1,osb+768);
          if constexpr(n==2) st16(PZ,c.rs,ov2,osb);
          if constexpr(n==3) st16(PZ,c.rs,hv1,hsb);
          if constexpr(n==4) st16(PZ,c.rs,hv1,hsb+2048);
          if constexpr(n==5) st16(PZ,c.rs,hv2,hsb+4096);
          if constexpr(n==6) st8(v2u{PZ.x,PZ.y},c.rs,sv,ssb);
          if constexpr(n==7){ asm volatile("" : "+v"(rcs)); float ct=rcs[0]; unsigned hi=pk2(ct,0.f); st4(pk2(ct,ct-blo(hi)),c.rs,csv,dcs); }
          if constexpr(n>=8 && n<13) st16(rf[(n-8)&7],c.rs,c.voff,d3+(n-8)*d3s);
          if constexpr(n==13) st16(rf[5],c.rs,c.voff,d4+(w&1)*2048);
          if constexpr(n==14) st16(rf[6],c.rs,c.voff,d4+(w&1)*2048+1024);
          if constexpr(n==15) st16(rf[7],c.rs,c.voff,(w&1)? d4+4096:WS_END); }
        if constexpr(I==27){ asm volatile("" : "+a"(acs[0]),"+a"(acs[1])); for(int a=0;a<2;a++) *(LAS float*)(csw+a*512)=acs[a][0]; }
        if constexpr(I==B2){ vmwait<5+(B2>K0? (B2-K0+KS-1)/KS:0)>(); barrier(); if(w==2) st8(c.tag,c.rs,c.flagv,WS_FLAG+FK_B*256*128); stamp(ws,c.b,30); }
        if constexpr(I>=W1 && (I-W1)%3==0 && (I-W1)/3<5) wop(IC<((I-W1)/3)%25>{});
        if constexpr(I==K0-1){ vmwait<21>(); asm volatile("" : "+v"(pf),"+v"(xr0),"+v"(xr1),"+v"(xr2));
          for(int sp=0;sp<SPIN_CAP;++sp){
            if(!__any(((pf.x^c.tag.x)|(pf.y^c.tag.y))!=0)) break;
            ld8(pf,c.rs,pfo,WS_FLAG+(FK_A*256+jq)*128); vmwait<0>(); asm volatile("" : "+v"(pf)); } }
        if constexpr(I>=K0 && I<K0+16*KS && (I-K0)%KS==0) ldV<0>(kp[((I-K0)/KS)&15],c.rs,kvo,WS_KVP+(jq*16+(((I-K0)/KS)&15))*KVP_B);
        if constexpr(I==W1+2){ const unsigned e=tok-8*hh, one=sel(e&1,0x3f800000u,0x3f80u);
          IDN=v4u{sel(e>>1==0,one,0),sel(e>>1==1,one,0),sel(e>>1==2,one,0),sel(e>>1==3,one,0)}; pinV(IDN); }
        if constexpr(I>=B2 && I<B2+6) csstep(IC<2>{},IC<(I-B2+600)%6>{});
        if constexpr(I==W1+1){ for(int z=0;z<2;z++){ int ch=tok+32*z, qu=(ch*187)>>11, dp=ch-qu*11; kia[z]=sel(ch<55,M_KVI+(((w>>1)*22+2*dp+hh)*2+(w&1))*48+qu*8,M_DUM); PIN(kia[z]); } }
        if constexpr(I==W1+4 || I==W1+7){ constexpr int a=(I-W1-4)/3; const int h2=(tok>>2)&1, q=(tok&3)+4*(tok>>3);
          unsigned d=sel(q==12,20,21); d=sel(q<12 && !h2,a==0? 8+q:q-8,d); d=sel(q<8,4*a+8*h2+q,d); kra[a&1]=M_KVI+a*2112+d*96+hh*48; PIN(kra[a&1]); }
        if constexpr(I==W1+10){ const unsigned rb=M_WO+160*(16*hh+4*blk+q4);
          wb[0][0]=rb+16*p4+8*(p4>>1); wb[0][1]=wb[0][0]^8; wb[0][2]=rb+128+8*p4; for(int t=0;t<3;t++) PIN(wb[0][t]); }
        if constexpr(I==W1+11){ for(int t=0;t<3;t++){ wb[1][t]=wb[0][t]+61440; PIN(wb[1][t]); } }
      });
    for(int a=0;a<2;a++) mfma32vv(aq[a],cf[a],MB1);
    stamp(ws,c.b,15);
    UNROLL(3,J,wop(IC<5+J>{}));
    f32x16 pq[2], ks; v4u q0[2],q1[2]; unsigned km=0;
    asm volatile("s_nop 3" : "+v"(aq[0]),"+v"(aq[1]));
    for(int a=0;a<2;a++){ q0[a]=pack8r(aq[a],0); q1[a]=v4u{pk2(aq[a][8],aq[a][9]),pk2(aq[a][10],aq[a][11]),0,0}; }
    for(int a=0;a<2;a++){ pinV(q0[a]); pinV(q1[a]); }
    nop2();
    for(int a=0;a<2;a++){ mfma32vv_0(pq[a],PW_[2+a][0],q0[a]); mfma32vv(pq[a],PW_[2+a][1],q1[a]); }
    auto red=[&](auto i) AI { constexpr int I=i; if constexpr(I==0) mfma32vv_0(ks,IDN,kp[0]); else mfma32vv(ks,IDN,kp[I]);
      umax3(km,kp[I].x,kp[I].z); };
    vmwait<3+10>(); red(IC<0>{}); red(IC<1>{}); red(IC<2>{});
    UNROLL(2,J,wop(IC<8+J>{}));
    asm volatile("s_nop 1" : "+v"(pq[0]),"+v"(pq[1]));
    hedgehog2(pq[0],pq[1],hh,rstd1*LOG2E,phq,[&](int k) AI {
      if(k==0){ wop(IC<10>{}); wop(IC<11>{}); }
      if(k==1){ wop(IC<12>{}); wop(IC<13>{}); wop(IC<14>{}); }
      if(k==2){ wop(IC<15>{}); wop(IC<16>{}); wop(IC<17>{}); }
      if(k==3){ wop(IC<18>{}); wop(IC<19>{}); } },
      [&](auto m) AI { constexpr int M=m; if constexpr(M==3) vmwait<7>(); red(IC<3+M>{}); });
    for(int a=0;a<2;a++){ pb[a][0]=v4u{phq[a][0],phq[a][1],phq[a][2],phq[a][3]}; pb[a][1]=v4u{phq[a][4],phq[a][5],phq[a][6],phq[a][7]}; pb[a][2]=v4u{phq[a][8],phq[a][9],0,0}; for(int s=0;s<3;s++) pinV(pb[a][s]); }
    stamp(ws,c.b,6);
    for(int sp=0;sp<SPIN_CAP;++sp){
      if(!__any(km==POISON)) break;
      c.nretry+=1<<24;
      UNROLL(16,I,ldV<0>(kp[I],c.rs,kvo,WS_KVP+(jq*16+I)*KVP_B)); vmwait<0>();
      km=0; PIN(km); UNROLL(16,I,red(IC<I>{})); mdone(ks);
    }
    stamp(ws,c.b,23);
    asm volatile("" : "+v"(ks));
    *(LAS v2u*)kia[0]=v2u{pk2(ks[0],ks[1]),pk2(ks[2],ks[3])}; *(LAS v2u*)kia[1]=v2u{pk2(ks[4],ks[5]),pk2(ks[6],ks[7])};
    ld8(pb2,c.rs,pbo,WS_FLAG+FK_B*256*128); ld8(pc2,c.rs,pco,WS_FLAG+FK_B*256*128);
    vmwait<7>(); lgk0(); barrier();
  }
  stamp(ws,c.b,24);
  { v4u ka[2][3];
    for(int a=0;a<2;a++) for(int s=0;s<3;s++) ka[a][s]=*(const LAS v4u*)(kra[a]+16*s);
    SB0();
    f32x16 o[2];
    for(int a=0;a<2;a++){ mfma32vv_0(o[a],ka[a][0],pb[a][0]); mfma32vv(o[a],ka[a][1],pb[a][1]); mfma32vv(o[a],ka[a][2],pb[a][2]); }
    mdone(o[1]); asm volatile("" : "+v"(o[0]));
    float r[2]; for(int a=0;a<2;a++) r[a]=__builtin_amdgcn_rcpf(o[a][12]+1e-6f);
    SB0();
    f32x2 y[2][6]; for(int a=0;a<2;a++) for(int e=0;e<6;e++) y[a][e]=f32x2{o[a][2*e],o[a][2*e+1]}*r[a];
    SB0();
    unsigned u_[2][6]; for(int a=0;a<2;a++) for(int e=0;e<6;e++) u_[a][e]=pk2(y[a][e][0],y[a][e][1]);
    st16(v4u{u_[0][0],u_[0][1],u_[0][2],u_[0][3]},c.rs,ov1,osb); st16(v4u{u_[1][0],u_[1][1],u_[1][2],u_[1][3]},c.rs,ov1,osb+768); st16(v4u{u_[0][4],u_[0][5],u_[1][4],u_[1][5]},c.rs,ov2,osb);
  }
  stamp(ws,c.b,7);
  v4u hr0,hr1; v2u hr2;
  { unsigned pr, prv=tk*1280+512+240;
    v4u WB[4][5]; v4u O[20]; const unsigned oo=WS_O+t16*20480;
    auto oload=[&](auto i) AI { constexpr int s=i; int sp=s+orot; sp= sp>=20? sp-20:sp; ldV<0>(O[s],c.rs,c.voff,oo+sp*1024); };
    UNROLL(5,I,wop(IC<20+I>{}));
    vmwait<8>(); asm volatile("" : "+v"(pb2),"+v"(pc2));
    for(int sp=0;sp<SPIN_CAP;++sp){
      if(!__any(((pb2.x^c.tag.x)|(pb2.y^c.tag.y)|(pc2.x^c.tag.x)|(pc2.y^c.tag.y))!=0)) break;
      c.nretry+=1<<24;
      ld8(pb2,c.rs,pbo,WS_FLAG+FK_B*256*128); ld8(pc2,c.rs,pco,WS_FLAG+FK_B*256*128); vmwait<0>(); asm volatile("" : "+v"(pb2),"+v"(pc2)); }
    v4u WA[12][5];
    auto pre=[&](auto i) AI { constexpr int s=i;
      if constexpr(s==12) barrier();
      #pragma unroll
      for(int u=0;u<5;u++){
        if constexpr(s<12){ unsigned a=wb[0][u<4? u&1:2]+s*5120+(u<4? 64*(u>>1):0); WA[s][u]=trfrag(a,a+1280); }
        else if constexpr(s<16){ unsigned a=wb[1][u<4? u&1:2]+(s-12)*5120+(u<4? 64*(u>>1):0); WB[s-12][u]=trfrag(a,a+1280); } }
      SB0();
      if constexpr(s<RP3+RP4) issue_piece<s>(c);
      if constexpr(OPR && s==OPR) asm volatile("buffer_load_dword %0, %1, %2, %3 offen sc1" : "=v"(pr) : "v"(prv), "s"(c.rs), "s"(oo) : "memory");
      #pragma unroll
      for(int u=0;u<5;u++){ if constexpr(s>=1 && s<13) pinA(WA[s-1][u]); else if constexpr(s>=13) pinV(WB[s-13][u]); }
      SB0(); };
    UNROLL(17,I,pre(IC<I>{}));
    if constexpr(ODLY>0) __builtin_amdgcn_s_sleep(ODLY);
    stamp(ws,c.b,16);
    auto again=[&]() AI {
      for(int s2=0;s2<SPIN_CAP;++s2){
        asm volatile("buffer_load_dword %0, %1, %2, %3 offen sc1\n\ts_waitcnt vmcnt(0)" : "=v"(pr) : "v"(prv), "s"(c.rs), "s"(oo) : "memory");
        if(!__any(pr==POISON)) break;
        c.nretry+=1<<28;
        if constexpr(PSLP>0) __builtin_amdgcn_s_sleep(PSLP); }
      UNROLL(12,J,oload(IC<J>{})); };
    if constexpr(OPR){ vmwait<RP3+RP4+1>(); barrier(); vmwait<0>(); PIN(pr);
      if(__any(pr==POISON)){ for(int s2=0;s2<SPIN_CAP;++s2){
        asm volatile("buffer_load_dword %0, %1, %2, %3 offen sc1\n\ts_waitcnt vmcnt(0)" : "=v"(pr) : "v"(prv), "s"(c.rs), "s"(oo) : "memory");
        if(!__any(pr==POISON)) break;
        __builtin_amdgcn_s_sleep(PSLP); } }
      UNROLL(12,J,oload(IC<J>{})); }
    else { again(); barrier(); }
    stamp(ws,c.b,17);
    f32x4 a2[5];
    v4u WC[4][5];
    unsigned omax;
    auto p2step=[&](auto i) AI { constexpr int I=i, s=I/5, u=I%5;
      if constexpr(u==0 && s%2==0) vmwait<(s<=9+RP2? 10:19+RP2-s)>();
      if constexpr(s==0) mfma16a_0(a2[u],WA[0][u],O[0]);
      else if constexpr(s<12) mfma16a(a2[u],WA[s<12?s:0][u],O[s]);
      else if constexpr(s<16) mfma16(a2[u],WB[(s-12)&3][u],O[s]);
      else mfma16(a2[u],WC[(s-16)&3][u],O[s]);
      SB0();
      if constexpr(u==1){ if constexpr(s<8) oload(IC<(12+s)%20>{}); else if constexpr(s==8) ld8(wf1,c.rs,wfo1,WS_FLAG+FK_B*256*128); else if constexpr(s<9+RP2) issue_piece<(s+RP3+RP4-9)>(c); }
      if constexpr(u==3 && s%2==0) asm volatile("v_max3_u32 %0, %0, %1, %2" : "+v"(omax) : "v"(O[s].x), "v"(O[s+1].x));
      if constexpr(I>=20 && I<60 && I%2==0){ constexpr int n=(I-20)/2, s2=n/5, u2=n%5; unsigned a=wb[1][u2<4? u2&1:2]+(4+s2)*5120+(u2<4? 64*(u2>>1):0); WC[s2][u2]=trfrag(a,a+1280); }
      SB0(); };
    for(int sp=0;;++sp){
      omax=0;
      UNROLL(100,I,p2step(IC<I>{}));
      if(!__any(omax==POISON) || sp>=REDO_CAP) break;
      vmwait<0>(); if constexpr((RP3+RP4)%5!=0 && (RP3+RP4)%5!=4) issue_piece<(RP3+RP4)/5*5>(c);
      again(); vmwait<0>(); UNROLL(12,J,pinV(O[J]));
    }
    f32x2 xf[10]; { unsigned xw[10]={xr0.x,xr0.y,xr0.z,xr0.w,xr1.x,xr1.y,xr1.z,xr1.w,xr2.x,xr2.y}; for(int i=0;i<10;i++) xf[i]=f32x2{blo(xw[i]),bhi(xw[i])}; }
    SB0();
    asm volatile("" : "+v"(a2[0]),"+v"(a2[1]),"+v"(a2[2]),"+v"(a2[3]),"+v"(a2[4]));
    stamp(ws,c.b,26);
    const bool sw=g4>=2;
    float hv_[20];
    for(int r=0;r<4;r++){ hv_[r]= sw? a2[1][r]:a2[0][r]; hv_[4+r]= sw? a2[0][r]:a2[1][r]; hv_[8+r]= sw? a2[3][r]:a2[2][r]; hv_[12+r]= sw? a2[2][r]:a2[3][r]; hv_[16+r]=a2[4][r]; }
    SB0();
    for(int i=0;i<10;i++) xf[i]+=f32x2{hv_[2*i],hv_[2*i+1]};
    SB0();
    unsigned hw[10]; for(int i=0;i<10;i++) hw[i]=pk2(xf[i][0],xf[i][1]);
    hr0=v4u{hw[0],hw[1],hw[2],hw[3]}; hr1=v4u{hw[4],hw[5],hw[6],hw[7]}; hr2=v2u{hw[8],hw[9]};
    st16(hr0,c.rs,hv1,hsb); st16(hr1,c.rs,hv1,hsb+2048);
    { auto sx=__builtin_amdgcn_permlane16_swap(hr2.x,hr2.x,false,false); auto sy=__builtin_amdgcn_permlane16_swap(hr2.y,hr2.y,false,false);
      st16(v4u{sx[0],sy[0],sx[1],sy[1]},c.rs,hv2,hsb+4096); }
    float s1=0.f,s1b=0.f; for(int i=0;i<5;i++){ s1=dot2(hw[2*i],C11,s1); s1b=dot2(hw[2*i+1],C11,s1b); }
    for(int i=0;i<10;i++) xf[i]=f32x2{blo(hw[i]),bhi(hw[i])};
    s1=xh_add(x16_add(s1+s1b))*(1.f/80.f);
    SB0();
    for(int i=0;i<10;i++) xf[i]-=s1;
    SB0();
    f32x2 s2={0.f,0.f}, s3={0.f,0.f}; for(int i=0;i<5;i++){ s2=__builtin_elementwise_fma(xf[2*i],xf[2*i],s2); s3=__builtin_elementwise_fma(xf[2*i+1],xf[2*i+1],s3); }
    s2+=s3;
    float s2t=xh_add(x16_add(s2[0]+s2[1]));
    st8(v2u{__float_as_uint(s1),__float_as_uint(s2t)},c.rs,sv,ssb);
    stamp(ws,c.b,18);
  }
  vmwait<4+RP2>(); asm volatile("" : "+v"(wf1));
  UNROLL(NPRE-RP3-RP4-RP2,I,issue_piece<RP3+RP4+RP2+I>(c));
  for(int sp=0;sp<SPIN_CAP;++sp){
    if(!__any(((wf1.x^c.tag.x)|(wf1.y^c.tag.y))!=0)) break;
    c.nretry+=1<<24;
    ld8(wf1,c.rs,wfo1,WS_FLAG+FK_B*256*128); vmwait<0>(); asm volatile("" : "+v"(wf1));
  }
  for(int i=0;i<3;i++){ c.lp[i]=lane*16 + i*65536; c.lq[i]=lane*16 + i*61440 + kh*5120; asm volatile("" : "+v"(c.lp[i]), "+v"(c.lq[i])); }
  c.actst = WS_ACT + ((pi*4+w)*2+blk)*55296 + hh*256 + tk*16; PIN(c.actst);
  c.actld = WS_ACT + (pi*8+rh*4+2*th)*55296 + kh*1024;
  c.av[0]=lane*16+kh*55296; c.av[1]=lane*16+(1-kh)*55296; PIN(c.av[0]); PIN(c.av[1]);
  v2u stt[4];
  c.xo = WS_H + (pi*4+w)*40960; c.rot=cc; c.pollv=(pi*16+tk)*128; PIN(c.pollv);
  unsigned stv=(4*hh)*1024+(32*w+tok)*8; PIN(stv);
  stamp(ws,c.b,19);
  bool first=true;
  auto window=[&]() AI {
    for(int sp=0;sp<SPIN_CAP;++sp){
      for(int q=0;q<4;q++) ld8(stt[q],c.rs,stv,WS_ST+pi*8192+q*1024);
      vmwait<0>();
      if(FB1 && first){ barrier(); first=false; }
      asm volatile("" : "+v"(stt[0]),"+v"(stt[1]),"+v"(stt[2]),"+v"(stt[3]));
      if(!__any(max(max(stt[0].x,stt[1].x),max(stt[2].x,stt[3].x))==POISON)) break;
      c.nretry+=1;
      if constexpr(PSLP>0) __builtin_amdgcn_s_sleep(PSLP);
    }
    UNROLL(XPRE,I,xload<I>(c)); };
  Epi ew;
  for(int sp=0;;++sp){
    window();
    if(!FB1 && sp==0) barrier();
    for(int r=0;r<RA;r++) c.A[r]=ldsA(c,r);
    { float m0=__uint_as_float(stt[0].x),m1=__uint_as_float(stt[1].x),m2=__uint_as_float(stt[2].x),m3=__uint_as_float(stt[3].x);
      float mean=xh_add((m0+m1)+(m2+m3))*0.125f;
      float q=(__uint_as_float(stt[0].y)+__uint_as_float(stt[1].y))+(__uint_as_float(stt[2].y)+__uint_as_float(stt[3].y));
      q += 80.f*(((m0-mean)*(m0-mean)+(m1-mean)*(m1-mean))+((m2-mean)*(m2-mean)+(m3-mean)*(m3-mean)));
      q = xh_add(q);
      c.var=q*(1.f/640.f)+1e-5f; float rs=__builtin_amdgcn_rsqf(c.var); c.c1r=GC1*rs; c.c2r=GC2*rs*rs*rs;
      unsigned mb=pk2(-mean,-mean); c.MB=v4u{mb,mb,mb,mb}; pinV(c.MB); PIN(c.var); PIN(c.c1r); PIN(c.c2r);
    }
    c.xmax=0;
    vmwait<XPRE-1>();
    SB0();
    stamp(ws,c.b,20);
    UNROLL(G1,I,gapx<I>(c,ew));
    if(__builtin_expect(!__any(c.xmax==POISON) || sp>=REDO_CAP,1)) break;
    c.nretry+=1;
    vmwait<0>(); lgk0();
  }
  UNROLL(GN-G1,I,gapx<G1+I>(c,ew));
  stamp(ws,c.b,21);
  { f32x2 hf[10]; { unsigned hw[10]={hr0.x,hr0.y,hr0.z,hr0.w,hr1.x,hr1.y,hr1.z,hr1.w,hr2.x,hr2.y}; for(int i=0;i<10;i++) hf[i]=f32x2{blo(hw[i]),bhi(hw[i])}; }
    SB0();
    asm volatile("" : "+v"(c.acc4[1][0]),"+v"(c.acc4[1][1]),"+v"(c.acc4[1][2]),"+v"(c.acc4[1][3]),"+v"(c.acc4[1][4]));
    for(int u=0;u<5;u++) *(LAS f32x4*)(c.lp[0]+w*5120+u*1024) = c.acc4[1][u];
    asm volatile("" : "+v"(c.acc4[0][0]),"+v"(c.acc4[0][1]),"+v"(c.acc4[0][2]),"+v"(c.acc4[0][3]),"+v"(c.acc4[0][4]));
    for(int i=0;i<10;i++) hf[i]+=f32x2{c.acc4[0][i/2][2*(i&1)],c.acc4[0][i/2][2*(i&1)+1]};
    const v4u rout=mkrs(ar.out,S*D*2);
    lgk0(); barrier();
    f32x4 r[5]; for(int u=0;u<5;u++) r[u]=*(const LAS f32x4*)(c.lp[0]+(w^1)*5120+u*1024);
    unsigned ow[10];
    for(int i=0;i<10;i++){ f32x2 y=hf[i]+f32x2{r[i/2][2*(i&1)],r[i/2][2*(i&1)+1]}; ow[i]=pk2(y[0],y[1]); }
    const unsigned so=t16*20480+cc*160, vo=tk*1280+g4*16;
    asm volatile("buffer_store_dwordx4 %0, %1, %2, %3 offen" :: "v"(v4u{ow[0],ow[1],ow[2],ow[3]}), "v"(vo), "s"(rout), "s"(so) : "memory");
    asm volatile("buffer_store_dwordx4 %0, %1, %2, %3 offen offset:64" :: "v"(v4u{ow[4],ow[5],ow[6],ow[7]}), "v"(vo), "s"(rout), "s"(so) : "memory");
    asm volatile("buffer_store_dwordx2 %0, %1, %2, %3 offen offset:128\n\ts_nop 1" :: "v"(v2u{ow[8],ow[9]}), "v"(vo-g4*8), "s"(rout), "s"(so) : "memory");
  }
  if(lid==0 && w==0) st8(c.tag,c.rs,c.flagv,WS_EPOCH);
  asm volatile(".Lhg_code_end:");
  stamp(ws,c.b,22);
  { const unsigned t0_=(unsigned)c.stt, t1_=(unsigned)c.st2; unsigned* pa_=(unsigned*)(ar.ws+WS_END+4096);
    unsigned te=(unsigned)__builtin_amdgcn_s_memrealtime();
    if(w==0){ __hip_atomic_store(pa_+blockIdx.x, t0_, RLX); __hip_atomic_store(pa_+1024+blockIdx.x, t1_, RLX); __hip_atomic_store(pa_+2048+blockIdx.x, te, RLX); }
    for(int i=0;i<100000 && (unsigned)__builtin_amdgcn_s_memrealtime()-te<500;i++) ;
    int mn=1<<30, sm_=0, ex=-(1<<30); const int l_=c.voff>>4;
    for(int q=0;q<4;q++){ mn=min(mn,(int)(__hip_atomic_load(pa_+q*64+l_,RLX)-t0_)); sm_+=(int)(__hip_atomic_load(pa_+1024+q*64+l_,RLX)-t0_); ex=max(ex,(int)(__hip_atomic_load(pa_+2048+q*64+l_,RLX)-t0_)); }
    for(int o=32;o>0;o>>=1){ mn=min(mn,__shfl_xor(mn,o)); sm_+=__shfl_xor(sm_,o); ex=max(ex,__shfl_xor(ex,o)); }
    int d=sm_/256-mn; d=min(max(d,0),10000);
    unsigned tgt=(unsigned)mn+t0_+5000+d;
    for(int i=0;i<400000 && (int)((unsigned)__builtin_amdgcn_s_memrealtime()-tgt)<0;i++) ;
  }
}

extern "C" void kernel_launch(void* const* d_in, const int* in_sizes, int n_in,
                              void* d_out, int out_size, void* d_ws, size_t ws_size,
                              hipStream_t stream) {
  Args a; a.x=(const u16*)d_in[0]; a.wqkv=(const u16*)d_in[1]; a.wphi=(const u16*)d_in[2]; a.wo=(const u16*)d_in[3];
  a.wg=(const u16*)d_in[5]; a.wu=(const u16*)d_in[6]; a.wd=(const u16*)d_in[7]; a.out=(u16*)d_out; a.ws=(char*)d_ws;
  hipFuncSetAttribute((const void*)blk_k, hipFuncAttributeMaxDynamicSharedMemorySize, 163840);
  for(int r=0;r<REPS;r++) blk_k<<<256,256,163840,stream>>>(a);
}
```

```cpp
#include <hip/hip_runtime.h>
#include <stdint.h>

typedef unsigned short u16;
typedef unsigned long long u64;
typedef unsigned v4u __attribute__((ext_vector_type(4)));
typedef unsigned v2u __attribute__((ext_vector_type(2)));
typedef float f32x16 __attribute__((ext_vector_type(16)));
typedef float f32x4 __attribute__((ext_vector_type(4)));
typedef float f32x2 __attribute__((ext_vector_type(2)));
typedef __bf16 bf2 __attribute__((ext_vector_type(2)));
typedef short s4 __attribute__((ext_vector_type(4)));
#define LAS __attribute__((address_space(3)))
#define DI __device__ __forceinline__

constexpr int S=2048, D=640, NH=32, DH=20, F=1728;

constexpr unsigned WS_EPOCH = 0;
constexpr unsigned WS_FLAG  = 4096;
constexpr unsigned WS_KVP   = WS_FLAG + 6*256*128;
constexpr unsigned WS_O     = WS_KVP + 256*3584;
constexpr unsigned WS_H     = WS_O + 128*20480;
constexpr unsigned WS_ST    = WS_H + 64*40960;
constexpr unsigned WS_ACT   = WS_ST + 16*8*128*8;
constexpr unsigned WS_WT3   = WS_ACT + 128*55296;
constexpr unsigned WS_CS3   = WS_WT3 + 16*286720;
constexpr unsigned WS_WT4   = WS_CS3 + 16*8192;
constexpr unsigned WS_END   = WS_WT4 + 8*286720;

static DI unsigned pk2(float a, float b){ f32x2 v={a,b}; return __builtin_bit_cast(unsigned, __builtin_convertvector(v, bf2)); }
#include <utility>
#define SB0() __builtin_amdgcn_sched_barrier(0)
#define AI __attribute__((always_inline))
#define UNROLL(N, I, ...) [&]<int... I>(std::integer_sequence<int,I...>) AI { (__VA_ARGS__, ...); }(std::make_integer_sequence<int,N>{})
template<int N> DI void vmwait(){ asm volatile("s_waitcnt vmcnt(%0)" :: "n"(N>63?63:N) : "memory"); }
DI void barrier(){ asm volatile("s_barrier" ::: "memory"); }
enum { FK_A=0, FK_B, FK_D0 };
DI void dma1(v4u rs, unsigned lds, unsigned voff, unsigned soff){
  asm volatile("s_mov_b32 m0, %0\n\ts_nop 0\n\tbuffer_load_dwordx4 %1, %2, %3 offen sc1 lds" :: "s"(lds), "v"(voff), "s"(rs), "s"(soff) : "memory");
}
template<int OFF> DI void ldA(v4u& d, v4u rs, unsigned voff, unsigned soff){
  asm volatile("buffer_load_dwordx4 %0, %1, %2, %3 offen offset:%4 sc1" : "=a"(d) : "v"(voff), "s"(rs), "s"(soff), "n"(OFF) : "memory");
}
template<int OFF> DI void ldV(v4u& d, v4u rs, unsigned voff, unsigned soff){
  asm volatile("buffer_load_dwordx4 %0, %1, %2, %3 offen offset:%4 sc1" : "=v"(d) : "v"(voff), "s"(rs), "s"(soff), "n"(OFF) : "memory");
}
DI void st16(v4u d, v4u rs, unsigned voff, unsigned soff){
  asm volatile("buffer_store_dwordx4 %0, %1, %2, %3 offen sc1\n\ts_nop 1" :: "v"(d), "v"(voff), "s"(rs), "s"(soff) : "memory");
}
DI void mfma32_0(f32x16& c, v4u a, v4u x){ asm volatile("v_mfma_f32_32x32x16_bf16 %0, %1, %2, 0" : "=&v"(c) : "v"(a), "a"(x)); }
DI void mfma32(f32x16& c, v4u a, v4u x){ asm volatile("v_mfma_f32_32x32x16_bf16 %0, %1, %2, %0" : "+v"(c) : "v"(a), "a"(x)); }
DI void mfma32av(f32x16& c, v4u a, v4u x){ asm volatile("v_mfma_f32_32x32x16_bf16 %0, %1, %2, %0" : "+v"(c) : "a"(a), "v"(x)); }
DI void mfma16_0(f32x4& c, v4u a, v4u x){ asm volatile("v_mfma_f32_16x16x32_bf16 %0, %1, %2, 0" : "=&v"(c) : "v"(a), "v"(x)); }
DI void mfma16(f32x4& c, v4u a, v4u x){ asm volatile("v_mfma_f32_16x16x32_bf16 %0, %1, %2, %0" : "+v"(c) : "v"(a), "v"(x)); }
DI void mfma16a(f32x4& c, v4u a, v4u x){ asm volatile("v_mfma_f32_16x16x32_bf16 %0, %1, %2, %0" : "+v"(c) : "a"(a), "v"(x)); }
constexpr int SPIN_BUDGET = 1<<18;
DI float blo(unsigned x){ return __uint_as_float(x<<16); }
DI float bhi(unsigned x){ return __uint_as_float(x&0xffff0000u); }
DI void pinA(v4u& x){ asm volatile("" : "+a"(x)); }
DI void pinV(v4u& x){ asm volatile("" : "+v"(x)); }
DI v2u trd(unsigned addr){ return __builtin_bit_cast(v2u, __builtin_amdgcn_ds_read_tr16_b64_v4i16((LAS s4*)addr)); }
DI v4u trfrag(unsigned a0, unsigned a1){ v2u x=trd(a0), y=trd(a1); return v4u{x.x,x.y,y.x,y.y}; }
DI v4u mkrs(const void* p, unsigned bytes){ u64 q=(u64)p; return v4u{(unsigned)q,(unsigned)(q>>32)&0xffffu,bytes,0x00020000u}; }
DI void dma0(v4u rs, unsigned lds, unsigned voff, unsigned soff){
  asm volatile("s_mov_b32 m0, %0\n\ts_nop 0\n\tbuffer_load_dwordx4 %1, %2, %3 offen lds" :: "s"(lds), "v"(voff), "s"(rs), "s"(soff) : "memory");
}
DI void st8(v2u d, v4u rs, unsigned voff, unsigned soff){
  asm volatile("buffer_store_dwordx2 %0, %1, %2, %3 offen sc1" :: "v"(d), "v"(voff), "s"(rs), "s"(soff) : "memory");
}
DI void st4(unsigned d, v4u rs, unsigned voff, unsigned soff){
  asm volatile("buffer_store_dword %0, %1, %2, %3 offen sc1" :: "v"(d), "v"(voff), "s"(rs), "s"(soff) : "memory");
}
DI void ld8(v2u& d, v4u rs, unsigned voff, unsigned soff){
  asm volatile("buffer_load_dwordx2 %0, %1, %2, %3 offen sc1" : "=v"(d) : "v"(voff), "s"(rs), "s"(soff) : "memory");
}
DI void lgk0(){ asm volatile("s_waitcnt lgkmcnt(0)" ::: "memory"); }
DI float dot2(unsigned a, unsigned b, float c){ return __builtin_amdgcn_fdot2_f32_bf16(__builtin_bit_cast(bf2,a),__builtin_bit_cast(bf2,b),c,false); }
DI float xh_add(float v){ auto r=__builtin_amdgcn_permlane32_swap(__float_as_uint(v),__float_as_uint(v),false,false); return __uint_as_float(r[0])+__uint_as_float(r[1]); }
DI float x16_add(float v){ auto r=__builtin_amdgcn_permlane16_swap(__float_as_uint(v),__float_as_uint(v),false,false); return __uint_as_float(r[0])+__uint_as_float(r[1]); }
constexpr unsigned OOB = 0x80000000u;
constexpr unsigned KVP_B=3584;
constexpr float LOG2E=1.4426950408889634f;

constexpr int NPRE=35, SYL=200, WLD=310, TG1=35, TG3=38, TG4=37, TOP=70, QAL=280;
constexpr int PW = 2;
constexpr int G3 = 280, GE = 288, GN = GE+135, NSTG = 26, RA = 6, NA = 8;
constexpr int G1 = 40*PW, EP0 = G1+4, EPL = 7, XPRE = 12;
enum { OP_NONE=0, OP_XL, OP_DMA, OP_ST, OP_WL, OP_AL, OP_PL, OP_CS };
constexpr bool is_sync(int g){ return g<G3 ? (g%(g<SYL? 40:20)==0 && g>=G1) : (g>=GE && g<GE+120 && (g-GE)%20==0); }
constexpr int sync_stage(int g){ return g<G3 ? g/20 : 14+(g-GE)/10; }
constexpr int sync_need(int g){ int m = g<G3 ? g/20+(g<SYL? 2:1) : 14+(g-GE)/10+2; return m<NSTG? m : NSTG-1; }
constexpr int ep_start(int t){ int g=EP0; for(int i=1;i<=t;i++){ g+=4*EPL; if(i>=PW && g<40*i+44) g=40*i+44; } return g; }
constexpr int PLG[4] = {203, 250, GE+6, GE+52};
constexpr int PCG[4] = {PLG[0]+28, PLG[1]+28, PLG[2]+28, PLG[3]+28};
constexpr int FSG[4] = {160, 220, 260, GE+20};
constexpr int FST[4] = {1, 3, 4, 6};
struct Sched { int op[GN+1]; int ix[GN+1]; int cnt[GN+2]; int dmag[NSTG]; int stg[7]; int xg[40]; int ag[54]; int wg[15]; int cg[7]; int ept[GN+1]; };
constexpr int al_kind(int i){ return i<32 ? i/16 : i<40 ? 2 : 3; }
constexpr int cs_use(int t){ return t<PW ? G1-PW+t : 40*t+39; }
constexpr Sched make_sched(){
  Sched s{};
  for(int g=0;g<=GN;g++) s.ept[g]=-1;
  for(int t=0;t<6;t++){ int g0=ep_start(t); for(int e=0;e<4*EPL;e++) s.ept[g0+e]=t*4*EPL+e; int g=g0+4*EPL; s.op[g]=OP_ST; s.ix[g]=t; s.stg[t]=g; }
  s.op[GE-1]=OP_ST; s.ix[GE-1]=6; s.stg[6]=GE-1;
  for(int k=0;k<4;k++){ s.op[PLG[k]]=OP_PL; s.ix[PLG[k]]=k; }
  for(int m=0;m<NSTG;m++) s.dmag[m]=-1;
  for(int i=0;i<XPRE;i++) s.xg[i]=-1;
  int ddl[NSTG]={};
  for(int m=0;m<NSTG;m++) for(int q=0;q<GN;q++) if(is_sync(q) && sync_need(q)>=m){ ddl[m]=q; break; }
  int nx=XPRE, nd=NPRE, na=0, nw=0, nc=0, cur=0, now=0, busy=QAL+2*TOP;
  for(int g=0; g<GN; g++){
    if(is_sync(g)) cur=sync_stage(g);
    now += g<EP0 ? TG1 : g<G3 ? TG3 : g<GE ? 40 : TG4;
    if(busy<now) busy=now;
    if(s.op[g]!=OP_NONE){ busy+=34; continue; }
    if(busy-now>QAL) continue;
    int pick=OP_NONE;
    if(nx<40) pick=OP_XL;
    else {
      int dd=1<<20, da=1<<20, dc=1<<20;
      if(nd<NSTG*5 && nd/5<=cur+7) dd=ddl[nd/5];
      if(na<54 && g>=PCG[al_kind(na)]) da=GE+(na/2)*5;
      if(nc<7) dc=cs_use(nc);
      int dw = (nw<15 && g>=60) ? WLD : (1<<20);
      int mn = dd<da? dd:da; mn = mn<dc? mn:dc; mn = mn<dw? mn:dw;
      if(mn<(1<<20)) pick = mn==dc? OP_CS : mn==da? OP_AL : mn==dd? OP_DMA : OP_WL;
    }
    if(pick==OP_NONE) continue;
    s.op[g]=pick; busy+=TOP;
    if(pick==OP_XL){ s.ix[g]=nx; s.xg[nx]=g; nx++; }
    if(pick==OP_DMA){ s.ix[g]=nd; s.dmag[nd/5]=g; nd++; }
    if(pick==OP_AL){ s.ix[g]=na; s.ag[na]=g; na++; }
    if(pick==OP_WL){ s.ix[g]=nw; s.wg[nw]=g; nw++; }
    if(pick==OP_CS){ s.ix[g]=nc; s.cg[nc]=g; nc++; }
  }
  s.cnt[0]=0;
  for(int g=0;g<=GN;g++) s.cnt[g+1]=s.cnt[g]+(s.op[g]!=OP_NONE);
  return s;
}
constexpr Sched SC = make_sched();
constexpr bool sched_ok(){
  if(NPRE%5 || NPRE<25 || NPRE>35) return false;
  for(int g=0;g<GN;g++) if(is_sync(g) && SC.dmag[sync_need(g)]>=g) return false;
  for(int m=NPRE/5;m<NSTG;m++) if(SC.dmag[m]<0) return false;
  for(int s=XPRE;s<40;s++) if(SC.xg[s]>=PW*s-2) return false;
  for(int i=0;i<54;i++) if(SC.ag[i]<=0 || SC.ag[i]>=GE+(i/2)*5-1) return false;
  for(int i=0;i<15;i++) if(SC.wg[i]<=0 || SC.wg[i]>=SC.ag[49]) return false;
  for(int i=0;i<7;i++) if(SC.cg[i]<=0 || SC.cg[i]>=cs_use(i)) return false;
  for(int k=0;k<4;k++) if(SC.stg[FST[k]]>=FSG[k] || !is_sync(FSG[k]) || SC.op[PLG[k]]!=OP_PL) return false;
  for(int t=0;t<7;t++) if(SC.op[SC.stg[t]]!=OP_ST) return false;
  if(SC.stg[5]>=G3) return false;
  for(int k=0;k<3;k++) if(PCG[k]>PLG[k+1]) return false;
  return true;
}
static_assert(sched_ok(), "schedule");
constexpr int after(int g, int g0){ return SC.cnt[g]-SC.cnt[g0+1]; }

struct Ctx {
  v4u rs; unsigned lp[3], lq[3]; unsigned voff; int lane, w, b, pi, pj, budget; v2u tag;
  unsigned src3, src4, actst, actld, av[2], xo, wlo, pollv, flagv, rot, xmax;
  v4u X[40]; v4u CS[7]; v4u WL[15]; v4u MB; v4u A[NA];
  f32x16 acc[7]; f32x4 acc4[2][5]; v4u B[54]; v2u pf;
  float c1r, c2r, var; float t0,t1,e0,e1,v0,v1; unsigned pk[2][4];
};
constexpr int SLOT[8]={5,6,0,1,2,7,3,4};
constexpr int fpos(int f){ return SLOT[(f/20)%8]*20+f%20; }
DI v4u ldsA(const Ctx& c, int a){
  if(a<G3) return *(const LAS v4u*)(c.lp[fpos(a)/64] + (fpos(a)%64)*1024);
  int q=a-G3; int f=fpos(G3 + (q/5)*10 + q%5);
  return *(const LAS v4u*)(c.lq[f/60] + (f%60)*1024);
}
template<int M> DI void issue_piece(Ctx& c){
  constexpr int stg=M/5, p=M%5, pb = p<4 ? 0 : 4;
  unsigned lds = SLOT[stg%8]*20480 + pb*1024 + c.w*5120;
  unsigned so = (stg<14 ? c.src3 + stg*20480 : c.src4 + (stg-14)*20480) + pb*1024;
  if constexpr(stg==12 || stg==13) so = c.pj>=12 ? WS_END : so;
  if constexpr(p==pb) dma1(c.rs, lds, c.voff, so);
  else asm volatile("buffer_load_dwordx4 %0, %1, %2 offen offset:%3 sc1 lds" :: "v"(c.voff), "s"(c.rs), "s"(so), "n"(p*1024) : "memory");
}
constexpr float GC1 = -2.302208198f, GC2 = -0.1029432397f;
#define PIN(x) asm volatile("" : "+v"(x))
template<int T,int P,int STEP> DI void epi7(Ctx& c){
  constexpr int q=2*P;
  if constexpr(STEP==0){ c.t0=c.acc[T][q]*c.acc[T][q]; PIN(c.t0); c.t1=c.acc[T][q+1]*c.acc[T][q+1]; PIN(c.t1); c.v0=c.acc[T][q]*c.acc[T][q+8]; PIN(c.v0); }
  if constexpr(STEP==1){ c.t0=__builtin_fmaf(c.t0,c.c2r,c.c1r); PIN(c.t0); c.t1=__builtin_fmaf(c.t1,c.c2r,c.c1r); PIN(c.t1); c.v1=c.acc[T][q+1]*c.acc[T][q+9]; PIN(c.v1); }
  if constexpr(STEP==2){ c.t0*=c.acc[T][q]; PIN(c.t0); c.t1*=c.acc[T][q+1]; PIN(c.t1); }
  if constexpr(STEP==3){ c.e0=__builtin_amdgcn_exp2f(c.t0); PIN(c.e0); c.e1=__builtin_amdgcn_exp2f(c.t1); PIN(c.e1); }
  if constexpr(STEP==4){ c.e0=__builtin_fmaf(c.e0,c.var,c.var); PIN(c.e0); c.e1=__builtin_fmaf(c.e1,c.var,c.var); PIN(c.e1); }
  if constexpr(STEP==5){ c.e0=__builtin_amdgcn_rcpf(c.e0); PIN(c.e0); c.e1=__builtin_amdgcn_rcpf(c.e1); PIN(c.e1); }
  if constexpr(STEP==6){ c.v0*=c.e0; PIN(c.v0); c.v1*=c.e1; PIN(c.v1); c.pk[T&1][P]=pk2(c.v0,c.v1); PIN(c.pk[T&1][P]); }
}
struct Epi { f32x2 t[4], v[4]; };
template<int STEP> DI void epiw(Ctx& c, Epi& e){
  for(int i=0;i<4;i++){ const f32x2 a={c.acc[6][2*i],c.acc[6][2*i+1]};
    if constexpr(STEP==0){ e.t[i]=a*a; e.v[i]=a*f32x2{c.acc[6][2*i+8],c.acc[6][2*i+9]}; }
    if constexpr(STEP==1) e.t[i]=__builtin_elementwise_fma(e.t[i],f32x2{c.c2r,c.c2r},f32x2{c.c1r,c.c1r});
    if constexpr(STEP==2) e.t[i]*=a;
    if constexpr(STEP==3) e.t[i]=f32x2{__builtin_amdgcn_exp2f(e.t[i][0]),__builtin_amdgcn_exp2f(e.t[i][1])};
    if constexpr(STEP==4) e.t[i]=__builtin_elementwise_fma(e.t[i],f32x2{c.var,c.var},f32x2{c.var,c.var});
    if constexpr(STEP==5) e.t[i]=f32x2{__builtin_amdgcn_rcpf(e.t[i][0]),__builtin_amdgcn_rcpf(e.t[i][1])};
    if constexpr(STEP==6) e.v[i]*=e.t[i];
    if constexpr(STEP==7) c.pk[0][i]=pk2(e.v[i][0],e.v[i][1]); }
  if constexpr(STEP<7) asm volatile("" : "+v"(e.t[0]),"+v"(e.t[1]),"+v"(e.t[2]),"+v"(e.t[3]),"+v"(e.v[0]),"+v"(e.v[1]),"+v"(e.v[2]),"+v"(e.v[3]));
}
template<int T> DI void act_store(Ctx& c){
  unsigned so = T*8192 + c.pj*512;
  if(T==6 && c.pj>=12) so = WS_END;
  st16(v4u{c.pk[T&1][0],c.pk[T&1][1],c.pk[T&1][2],c.pk[T&1][3]}, c.rs, c.actst, so);
}
template<int ix> DI void xload(Ctx& c){ constexpr int e=ix%5; ldA<(e&3)*1024>(c.X[ix], c.rs, c.voff, c.xo + ((ix/5+c.rot)&7)*5120 + (e&4)*1024); }
template<int ix> DI void aload(Ctx& c){ constexpr int m=ix/2; ldV<(m&1)*2048>(c.B[ix], c.rs, c.av[ix&1], c.actld + (m/2)*4096); }
template<int g> DI void vmem_op(Ctx& c){
  constexpr int op=SC.op[g], ix=SC.ix[g];
  if constexpr(op==OP_XL) xload<ix>(c);
  if constexpr(op==OP_DMA) issue_piece<ix>(c);
  if constexpr(op==OP_ST) act_store<ix>(c);
  if constexpr(op==OP_WL) ldA<((ix%5)&3)*1024>(c.WL[ix], c.rs, c.voff, c.wlo + (ix/5)*10240 + ((ix%5)&4)*1024);
  if constexpr(op==OP_CS) ldA<(ix%4)*1024>(c.CS[ix], c.rs, c.voff, (ix==6 && c.pj>=12)? WS_END : WS_CS3 + c.pj*8192 + (ix/4)*4096);
  if constexpr(op==OP_AL) aload<ix>(c);
  if constexpr(op==OP_PL) ld8(c.pf, c.rs, c.pollv, WS_FLAG+(FK_D0+ix)*256*128);
}
constexpr unsigned POISON = 0xffffffffu;
DI void useA(v4u a, v4u b, v4u c, v4u d){ asm volatile("" :: "v"(a),"v"(b),"v"(c),"v"(d)); }
template<int g> DI void sync_pt(Ctx& c){
  if constexpr(is_sync(g)){
    constexpr int m=sync_need(g);
    constexpr int n1 = SC.dmag[m]>=0 ? after(g,SC.dmag[m]) : 63;
    constexpr int k = g==FSG[0]?0 : g==FSG[1]?1 : g==FSG[2]?2 : g==FSG[3]?3 : -1;
    constexpr int n2 = k>=0 ? after(g,SC.stg[FST[k<0?0:k]]) : 63;
    if constexpr((n1<n2?n1:n2)<63) vmwait<(n1<n2?n1:n2)>();
    barrier();
    if constexpr(k>=0){ if(c.w==k) st8(c.tag, c.rs, c.flagv, WS_FLAG+(FK_D0+k)*256*128); }
    SB0();
  }
}
template<int g> DI void gapx(Ctx& c, Epi& ew){
  if constexpr(g<G3){
    constexpr int T = g<G1 ? g%PW : g/40, s = g<G1 ? g/PW : g%40;
    if constexpr(g%4==0) useA(c.A[g%NA],c.A[(g+1)%NA],c.A[(g+2)%NA],c.A[(g+3)%NA]);
    if constexpr(s==0) mfma32_0(c.acc[T], c.A[g%NA], c.X[s]); else mfma32(c.acc[T], c.A[g%NA], c.X[s]);
    if constexpr(s==39){ vmwait<after(g,SC.cg[T])>(); mfma32av(c.acc[T], c.CS[T], c.MB); }
    SB0();
    sync_pt<g>(c);
    c.A[(g+RA)%NA] = ldsA(c, g+RA);
    vmem_op<g>(c);
    if constexpr(g<G1 && T==0){ pinA(c.X[s]); c.e0=__uint_as_float(c.X[s].x); PIN(c.e0); }
    if constexpr(g<G1 && T==1){ c.xmax=max(c.xmax,__float_as_uint(c.e0)); PIN(c.xmax); }
    if constexpr(g<G1 && T==PW-1 && s+1<40){ if constexpr(s+1<XPRE) vmwait<XPRE-2-s+SC.cnt[g+1]>(); else vmwait<after(g+1,SC.xg[s+1])>(); }
    if constexpr(SC.ept[g]>=0){ constexpr int e=SC.ept[g]; epi7<e/(4*EPL),(e/EPL)%4,e%EPL>(c); }
  } else if constexpr(g<GE){
    if constexpr(g==G3) asm volatile("s_nop 9" : "+v"(c.acc[6]));
    epiw<g-G3>(c,ew);
    vmem_op<g>(c);
  } else {
    constexpr int q=g-GE, m=q/5, u=q%5, a=G3+q;
    if constexpr(q<120 && q%4==0) useA(c.A[a%NA],c.A[(a+1)%NA],c.A[(a+2)%NA],c.A[(a+3)%NA]);
    if constexpr(q<120){
      if constexpr(m==0) mfma16_0(c.acc4[1][u], c.A[a%NA], c.B[1]); else mfma16(c.acc4[1][u], c.A[a%NA], c.B[2*m+1]);
      SB0();
      sync_pt<g>(c);
      vmem_op<g>(c);
      SB0();
      if constexpr(m==0) mfma16_0(c.acc4[0][u], c.A[a%NA], c.B[0]); else mfma16(c.acc4[0][u], c.A[a%NA], c.B[2*m]);
    } else { mfma16a(c.acc4[1][u], c.WL[q-120], c.B[2*m+1]); SB0(); vmem_op<g>(c); SB0(); mfma16a(c.acc4[0][u], c.WL[q-120], c.B[2*m]); }
    SB0();
    if constexpr(q+RA<120) c.A[(a+RA)%NA] = ldsA(c, a+RA);
  }
  if constexpr(g>=GE-1 && (g+1-GE)%5==0 && g+1<GN) vmwait<after(g+1,SC.ag[2*((g+1-GE)/5)+1])>();
  { constexpr int k = g+1==PCG[0]?0 : g+1==PCG[1]?1 : g+1==PCG[2]?2 : g+1==PCG[3]?3 : -1;
    if constexpr(k>=0){
      vmwait<after(g+1,PLG[k])>(); asm volatile("" : "+v"(c.pf));
      if(__builtin_expect(__any(((c.pf.x^c.tag.x)|(c.pf.y^c.tag.y))!=0),0)){
        _Pragma("nounroll") for(;;){ ld8(c.pf, c.rs, c.pollv, WS_FLAG+(FK_D0+k)*256*128); vmwait<0>(); asm volatile("" : "+v"(c.pf)); if(!__any(((c.pf.x^c.tag.x)|(c.pf.y^c.tag.y))!=0) || --c.budget<0) break; } }
    } }
  SB0();
}
constexpr int RP1=10, RP2=10, RT0=117, WQ0=135, WQS=3, WQN=5;
static_assert(RP1%5==0 && RP1<=10, "early ring pieces");
template<int I> using IC = std::integral_constant<int,I>;
template<int N,int DP,class RD,class MM,class SD> DI void pipe(RD rd, MM mm, SD sd){
  v4u A[DP+1];
  [&]<int... I>(std::integer_sequence<int,I...>) AI { ((A[I]=rd(IC<I>{})), ...); }(std::make_integer_sequence<int,DP>{});
  SB0();
  auto step=[&](auto i) AI { constexpr int I=i; if constexpr(I%2==0 && I+1<N) asm volatile("" :: "v"(A[I%(DP+1)]),"v"(A[(I+1)%(DP+1)])); mm(i,A[I%(DP+1)]); SB0(); sd(i); if constexpr(I+DP<N) A[(I+DP)%(DP+1)]=rd(IC<I+DP>{}); SB0(); };
  [&]<int... I>(std::integer_sequence<int,I...>) AI { (step(IC<I>{}), ...); }(std::make_integer_sequence<int,N>{});
}
DI void mfma32vv_0(f32x16& c, v4u a, v4u x){ asm volatile("v_mfma_f32_32x32x16_bf16 %0, %1, %2, 0" : "=&v"(c) : "v"(a), "v"(x)); }
DI void mfma32vv(f32x16& c, v4u a, v4u x){ asm volatile("v_mfma_f32_32x32x16_bf16 %0, %1, %2, %0" : "+v"(c) : "v"(a), "v"(x)); }
DI void mfma32A_0(f32x16& c, v4u a, v4u x){ asm volatile("v_mfma_f32_32x32x16_bf16 %0, %1, %2, 0" : "=a"(c) : "v"(a), "v"(x)); }
DI void mfma32A(f32x16& c, v4u a, v4u x){ asm volatile("v_mfma_f32_32x32x16_bf16 %0, %1, %2, %0" : "+a"(c) : "v"(a), "v"(x)); }
DI void mfma16a_0(f32x4& c, v4u a, v4u x){ asm volatile("v_mfma_f32_16x16x32_bf16 %0, %1, %2, 0" : "=&v"(c) : "a"(a), "v"(x)); }
DI void nop2(){ asm volatile("s_nop 1" ::: "memory"); }
DI void mdone(f32x16& c){ asm volatile("s_nop 11" : "+v"(c)); }
#define PINS(x) asm volatile("" : "+s"(x))
DI float max3(float a,float b,float c){ float r; asm("v_max3_f32 %0, %1, %2, %3" : "=v"(r) : "v"(a),"v"(b),"v"(c)); return r; }
DI float min3(float a,float b,float c){ float r; asm("v_min3_f32 %0, %1, %2, %3" : "=v"(r) : "v"(a),"v"(b),"v"(c)); return r; }
DI f32x2 xh(float v){ auto r=__builtin_amdgcn_permlane32_swap(__float_as_uint(v),__float_as_uint(v),false,false); return f32x2{__uint_as_float(r[0]),__uint_as_float(r[1])}; }
template<class H,class M> DI void hedgehog2(f32x16 p, f32x16 r, bool hh, float sc, unsigned (&o)[2][10], H hook, M mid){
  f32x16 z[2]={p,r};
  float a3[2][3], i3[2][3], mx[2], mn[2];
  for(int a=0;a<2;a++){ float x8=z[a][8],x9=z[a][9],xa=z[a][10],xb=z[a][11]; PIN(xa); PIN(xb); z[a][8]=hh? xa:x8; z[a][9]=hh? xb:x9; }
  for(int a=0;a<2;a++) for(int k=0;k<2;k++){ a3[a][k]=max3(z[a][3*k],z[a][3*k+1],z[a][3*k+2]); i3[a][k]=min3(z[a][3*k],z[a][3*k+1],z[a][3*k+2]); }
  SB0(); mid(IC<0>{}); SB0();
  for(int a=0;a<2;a++){ a3[a][2]=max3(z[a][6],z[a][7],z[a][8]); i3[a][2]=min3(z[a][6],z[a][7],z[a][8]); }
  for(int a=0;a<2;a++){ mx[a]=max3(a3[a][0],a3[a][1],z[a][9]); mn[a]=min3(i3[a][0],i3[a][1],z[a][9]); }
  SB0(); mid(IC<1>{}); SB0();
  for(int a=0;a<2;a++){ mx[a]=max3(mx[a],a3[a][2],a3[a][2]); mn[a]=min3(mn[a],i3[a][2],i3[a][2]); }
  SB0(); mid(IC<2>{}); SB0();
  f32x2 sx[2], sn_[2];
  for(int a=0;a<2;a++){ sx[a]=xh(mx[a]); sn_[a]=xh(mn[a]); }
  SB0();
  for(int a=0;a<2;a++){ mx[a]=max3(sx[a][0],sx[a][1],sx[a][1]); mn[a]=min3(sn_[a][0],sn_[a][1],sn_[a][1]); }
  SB0();
  for(int a=0;a<2;a++){ mx[a]*=-sc; mn[a]*=sc; }
  SB0(); hook(0); SB0();
  f32x2 ep[2][5], en[2][5];
  for(int a=0;a<2;a++) for(int i=0;i<5;i++){ f32x2 v={z[a][2*i],z[a][2*i+1]};
      ep[a][i]=__builtin_elementwise_fma(v,f32x2{sc,sc},f32x2{mx[a],mx[a]}); en[a][i]=__builtin_elementwise_fma(v,f32x2{-sc,-sc},f32x2{mn[a],mn[a]}); }
  SB0();
  asm volatile("" : "+v"(ep[0][0]),"+v"(ep[0][1]),"+v"(ep[0][2]),"+v"(ep[0][3]),"+v"(ep[0][4]),"+v"(en[0][0]),"+v"(en[0][1]),"+v"(en[0][2]),"+v"(en[0][3]),"+v"(en[0][4]));
  asm volatile("" : "+v"(ep[1][0]),"+v"(ep[1][1]),"+v"(ep[1][2]),"+v"(ep[1][3]),"+v"(ep[1][4]),"+v"(en[1][0]),"+v"(en[1][1]),"+v"(en[1][2]),"+v"(en[1][3]),"+v"(en[1][4]));
  auto ex=[&](auto J) AI { constexpr int a=J/5, i=J%5;
    mid(IC<3+J>{}); SB0();
    ep[a][i]=f32x2{__builtin_amdgcn_exp2f(ep[a][i][0]),__builtin_amdgcn_exp2f(ep[a][i][1])}; en[a][i]=f32x2{__builtin_amdgcn_exp2f(en[a][i][0]),__builtin_amdgcn_exp2f(en[a][i][1])};
    asm volatile("" : "+v"(ep[a][i]),"+v"(en[a][i]));
    SB0(); if constexpr(i==4){ hook(1+a); SB0(); } };
  UNROLL(10,J,ex(IC<J>{}));
  f32x2 s0[4], s1[4];
  for(int a=0;a<2;a++){ s0[a]=ep[a][0]+ep[a][1]; s1[a]=ep[a][2]+ep[a][3]; s0[2+a]=en[a][0]+en[a][1]; s1[2+a]=en[a][2]+en[a][3]; }
  SB0();
  for(int a=0;a<2;a++){ s0[a]+=ep[a][4]; s0[2+a]+=en[a][4]; }
  SB0();
  for(int k=0;k<4;k++) s0[k]+=s1[k];
  SB0();
  float t[4]; for(int k=0;k<4;k++) t[k]=s0[k][0]+s0[k][1];
  SB0();
  for(int k=0;k<4;k++) s0[k]=xh(t[k]);
  SB0();
  for(int k=0;k<4;k++) t[k]=s0[k][0]+s0[k][1];
  SB0();
  for(int k=0;k<4;k++) t[k]=__builtin_amdgcn_rcpf(t[k]);
  SB0(); hook(3); SB0();
  for(int a=0;a<2;a++) for(int i=0;i<5;i++){ ep[a][i]*=t[a]; en[a][i]*=t[2+a]; }
  SB0();
  for(int a=0;a<2;a++) for(int i=0;i<5;i++){ o[a][i]=pk2(ep[a][i][0],ep[a][i][1]); o[a][5+i]=pk2(en[a][i][0],en[a][i][1]); }
  SB0();
}
DI v4u pack8r(const f32x16& a, int r0){ return v4u{pk2(a[r0],a[r0+1]),pk2(a[r0+2],a[r0+3]),pk2(a[r0+4],a[r0+5]),pk2(a[r0+6],a[r0+7])}; }

struct Args { const u16 *x,*wqkv,*wphi,*wo,*wg,*wu,*wd; u16* out; char* ws; };
constexpr unsigned M_W=0, M_XS=49152, M_DUM=98304, M_WQ=102400, M_RT=24576, M_R4=M_RT+20480, M_PHI=63488, M_VI=M_PHI+128*168, M_WO=0;
constexpr unsigned M_CSX=153600, M_KVI=M_CSX+2048, M_WP=M_KVI+4224;
template<int OFF> DI void dmaN(v4u rs, unsigned voff, unsigned soff){
  asm volatile("buffer_load_dwordx4 %0, %1, %2 offen offset:%3 lds" :: "v"(voff), "s"(rs), "s"(soff), "n"(OFF) : "memory");
}
template<int OFF> DI void ldsrdA(v4u& d, unsigned a){ asm volatile("ds_read_b128 %0, %1 offset:%2" : "=a"(d) : "v"(a), "n"(OFF) : "memory"); }
template<int OFF> DI void ldAnt(v4u& d, v4u rs, unsigned voff, unsigned soff){
  asm volatile("buffer_load_dwordx4 %0, %1, %2, %3 offen offset:%4 nt" : "=a"(d) : "v"(voff), "s"(rs), "s"(soff), "n"(OFF) : "memory");
}
template<int OFF> DI void ldswrA(unsigned a, v4u d){ asm volatile("ds_write_b128 %0, %1 offset:%2" :: "v"(a), "a"(d), "n"(OFF) : "memory"); }
DI void umax3(unsigned& m, unsigned a, unsigned b){ asm volatile("v_max3_u32 %0, %0, %1, %2" : "+v"(m) : "v"(a), "v"(b)); }
DI unsigned sel(bool c, unsigned a, unsigned b){ return c? a:b; }
DI void touch(unsigned& d, v4u rs, unsigned voff, unsigned soff){
  asm volatile("buffer_load_dword %0, %1, %2, %3 offen" : "=v"(d) : "v"(voff), "s"(rs), "s"(soff) : "memory");
}

__global__ __launch_bounds__(256,1) void blk_k(Args ar){
  char* ws=ar.ws;
  Ctx c;
  c.budget=SPIN_BUDGET; c.b=blockIdx.x; c.lane=threadIdx.x&63; c.w=__builtin_amdgcn_readfirstlane(threadIdx.x>>6);
  c.pi=((c.b&7)>>2)*8+((c.b>>3)&7); c.pj=(c.b&3)*4+(c.b>>6);
  const int lane=c.lane, w=c.w, pi=c.pi, pj=c.pj, jq=pj^1, tid=threadIdx.x;
  unsigned dummy;
  { u64 pc; unsigned len; asm volatile("s_getpc_b64 %0\n.Lhg_code_begin:\n\ts_mov_b32 %1, .Lhg_code_end-.Lhg_code_begin" : "=s"(pc), "=s"(len));
    touch(dummy, mkrs((const void*)pc,len), sel(lane<4,lane*128,OOB), ((c.b>>3)*16+w*4)*128); }
  const int lid=pi*16+pj; const bool job = lid<216;
  const int jp=(lid*152)>>12, jl=lid-jp*27, ju=lid*2+(w>>1), js=ju>>3;
  const v4u rx=mkrs(ar.x,S*D*2), rw=mkrs(ar.wqkv,D*1920*2);
  const int rot=(3*pi+pj)%10; int kph=rot;
  const unsigned xs=M_XS+w*12288, xrow=(pi*128+w*32)*1280, wrow=w*16*3840+pj*80;
  const int g4=lane>>4, tk=lane&15;
  unsigned xv[4]; for(int e=0;e<4;e++) xv[e]=(lane>>3)*1280+(((lane&7)^(g4+4*(e&1))))*16+e*(10240-1024);
  unsigned wv[3];
  unsigned xso=xrow+kph*128, wso=wrow+kph*(64*3840);
  auto ldp=[&](int kc,auto P) AI { constexpr int p=P;
    if constexpr(p==0) dma0(rx, xs+(kc%3)*4096, xv[0], xso); else if constexpr(p<4) dmaN<p*1024>(rx,xv[p&3],xso);
    else if constexpr(p==4) dma0(rw, M_W+(kc%4)*12288+w*3072, wv[0], wso); else dmaN<(p-4)*1024>(rw,wv[(p-4)%3],wso);
    if constexpr(p==6){ kph = kph==9? 0:kph+1; xso=xrow+kph*128; wso=wrow+kph*(64*3840); } };
  UNROLL(4,I,ldp(0,IC<I>{}));
  for(int p=0;p<3;p++){ const unsigned idx=p*64+lane, n=(idx*171)>>11, ch=idx-n*12; unsigned cb=sel(ch<5,1280,2480); cb=sel(ch<10,cb,OOB); wv[p]=n*(3840-192)+lane*16+cb; }
  UNROLL(3,I,ldp(0,IC<4+I>{}));
  SB0();
  c.rs=mkrs(ws,WS_END); c.voff=lane*16;
  const int tok=lane&31, hh=lane>>5;
  const int cc=pj&7, rh=pj>>3, th=w>>1, kh=w&1, t16=pi*8+rh*4+w;
  const int blk=g4&1, q4=tk>>2, p4=lane&3, qd=blk*4+p4;
  v4u PZ={POISON,POISON,POISON,POISON}; pinV(PZ);
  const unsigned osb=WS_O+(pi*4+w)*40960+jq*1280, hsb=WS_H+(t16>>1)*40960+cc*5120+(t16&1)*256, ssb=WS_ST+pi*8192+cc*1024+(rh*64+w*16)*8;
  unsigned ov=blk*20480+tk*16, ov1=ov+hh*256, ov2=sel(hh,OOB,ov+512), kvo=sel(lane<55,w*896+lane*16,OOB);
  unsigned hv1=tk*16+g4*512, hv2=sel(blk,OOB,tk*16+hh*512), sv=sel(lane<16,tk*8,OOB);
  unsigned pfo=tk*2048, xo_=tk*1280+g4*16, xo2=xo_-g4*8;
  c.flagv=sel(lane==0,lid*128,OOB); PIN(c.flagv);
  PIN(ov1); PIN(ov2); PIN(kvo); PIN(hv1); PIN(hv2); PIN(sv); PIN(pfo); PIN(xo_); PIN(xo2);
  float sm[2]={0.f,0.f}, sq[2]={0.f,0.f};
  unsigned C11=0x3f803f80u; PIN(C11);
  unsigned xr[4]; { unsigned b0=xs+tok*128, x0=(hh^((tok>>1)&7))*16; for(int s=0;s<4;s++){ xr[s]=b0+(x0^(32*s)); PIN(xr[s]); } }
  v4u ONE; { unsigned o1=sel(tok==0,0x3f803f80u,0u); ONE=v4u{o1,o1,o1,o1}; pinV(ONE); }
  unsigned ab[3], aw[3];
  { const unsigned rb=M_W+192*(8*hh+q4);
    ab[0]=rb+8*(qd+sel(qd<5,0,8)); ab[1]=rb+8*(qd+5); ab[2]=rb+8*(16+qd);
    for(int t=0;t<3;t++){ aw[t]=ab[t]+w*3072; PIN(ab[t]); PIN(aw[t]); } }
  const unsigned qd4=8*min(qd,4);
  unsigned pwb=M_WP+q4*40+qd4, pwa=pwb+hh*160; PIN(pwa); PIN(pwb);
  unsigned csw=sel(hh,M_DUM,M_CSX)+lane*16+w*4, csr=M_CSX+tok*16; PIN(csw); PIN(csr);
  unsigned prow=M_PHI+(w*32+tok)*168+hh*40, vrow=M_VI+(w*32+tok)*104; PIN(prow);
  unsigned vwa[2], vwb[2];
  vwa[0]=vrow+40-16*hh; vwa[1]=vrow+88-88*hh; vwb[0]=vrow+32+16*hh; vwb[1]=vrow+8+8*hh;
  for(int t=0;t<2;t++){ PIN(vwa[t]); PIN(vwb[t]); }
  unsigned pa=M_PHI+(8*hh+q4)*168+w*40+qd4, va=M_VI+(8*hh+q4)*104+(w>>1)*48+8*min(qd,5), kvs=sel(tok<22,tok*8+hh*176,OOB), kvs2=sel(hh,OOB,kvs);
  PIN(pa); PIN(va); PIN(kvs); PIN(kvs2);
  *(LAS v2u*)(sel(tid<88,M_KVI+tid*48+40,M_DUM))=v2u{0,0};
  unsigned qv[5], ovv[5], rdv[3];
  for(int p=0;p<5;p++){ int idx=p*64+lane, n=(idx*205)>>10; int row=(n&~15)+4*(n&3)+((n>>2)&3); qv[p]=row*3840-n*80+lane*16+(p&4)*1024; PIN(qv[p]); }
  auto wqp=[&](auto N) AI { constexpr int n=N, p=n%5; const int grp=(n/5)*4+w; int gp=grp+rot; gp= gp>=10? gp-10:gp; const unsigned so=gp*(64*3840)+jq*80;
    if(n<10 || w<2){ if constexpr(p%4==0) dma0(rw,M_WQ+grp*5120+p*1024,qv[p],so); else { dma0(rw,M_WQ+grp*5120+p*1024,qv[p],so+p*1024); } } };
  SB0();
  v2u ep; ld8(ep,c.rs,0u,WS_EPOCH);
  if(lane<50) dma0(mkrs(ar.wphi,NH*400*2), M_WP+w*800, c.voff, (pj>>1)*3200+w*800);
  UNROLL(4,I,ldp(1,IC<I>{}));
  for(int p=0;p<5;p++){ int idx=p*64+lane, n=(idx*205)>>11; int row=(n&~12)|((n&4)<<1)|((n&8)>>1); ovv[p]=row*1280-n*160+lane*16+(p&4)*1024; PIN(ovv[p]);
    if(p<3){ rdv[p]=n*(1280-160)+lane*16; if(p==2) rdv[p]=sel(lane<32,rdv[p],OOB); PIN(rdv[p]); } }
  v4u ro=mkrs(ar.wo,D*D*2), rd=mkrs(ar.wd,job? D*F*2:0), rg=mkrs(w&1? ar.wu:ar.wg,job? D*F*2:0); PINS(ro); PINS(rd); PINS(rg);
  unsigned s3o=(jp*80+(w>>1)*40)*3456+jl*128, s4o=(js*32+(w&1)*16)*1280+(ju&7)*160, rtv=(lane>>3)*3456+(lane&7)*16;
  PINS(s3o); PINS(s4o); PIN(rtv);
  v4u RG[8];
  auto rtp=[&](auto P) AI { constexpr int p=P;
    if constexpr(p<5) ldAnt<0>(RG[p],rg,rtv,s3o+p*8*3456); else ldAnt<(p-5)*1024>(RG[p],rd,rdv[(p-5)%3],s4o); };
  unsigned rw3=M_RT+(w&1)*10240+(w>>1)*5120+lane*16, rw4=M_R4+w*3072+lane*16; PIN(rw3); PIN(rw4);
  unsigned pbo=(pi*16+tk)*128, pco=((lane&7)*27+4*min(lane>>3,pj<12? 6:5)+(pj>>2))*128; PIN(pbo); PIN(pco);
  unsigned wov=sel(lane<25,((lane*52)>>8)*640+lane*128,OOB); PIN(wov);
  const int orot=(5*cc)>>1;
  auto wop=[&](auto N) AI { constexpr int n=N, p=n%5; constexpr int rn=n/5; int s=4*rn+w, sp=s+orot; sp= sp>=20? sp-20:sp; unsigned so=sp*32*1280+cc*160;
    if constexpr(p%4==0) dma0(ro,M_WO+s*5120+p*1024,ovv[p],so); else dmaN<p*1024>(ro,ovv[p],so); };
  unsigned d3, d3s, dcs, d4, r3, r4a, r4b, csv=sel(hh,OOB,tok*16); PIN(csv);
  { r3=M_RT+blk*10240+(8*hh+q4)*128+8*(((p4&1)<<1)|(p4>>1))+w*32;
    const unsigned r4=M_R4+(w>>1)*6144+hh*3072+(8*blk+q4)*160; r4a=r4+64*(w&1)+16*p4; r4b=r4+128+8*p4; PIN(r3); PIN(r4a); PIN(r4b);
    const int tau=jl*4+w, j2=tau&15, i2=tau>>4;
    const int sl0=(jp-j2)&7;
    d3= job? WS_WT3+j2*286720+(i2<PW? PW*sl0*5+i2 : 40*i2+sl0*5)*1024 : WS_END; d3s= i2<PW? PW*1024:1024;
    dcs=job? WS_CS3+j2*8192+i2*1024+(jp>>2)*512+(jp&3)*4:WS_END;
    d4= job? WS_WT4+(ju&7)*286720+js*5120 : WS_END;
    PINS(d3); PINS(d3s); PINS(dcs); PINS(d4); }
  unsigned qb[2], qw[2]; for(int a=0;a<2;a++){ qb[a]=M_WQ+80*(4*q4+2*hh)+40*a+qd4; qw[a]=qb[a]+w*12800; PIN(qb[a]); PIN(qw[a]); }
  c.src3 = WS_WT3 + pj*286720 + w*5120;
  c.src4 = WS_WT4 + cc*286720 + w*5120;
  c.wlo = WS_WT4 + cc*286720 + 240*1024 + kh*5120;
  SB0();
  UNROLL(3,I,ldp(1,IC<4+I>{}));
  st16(PZ,c.rs,kvo,WS_KVP+(pj*16+pi)*KVP_B);
  f32x16 acc[3], acs[3]; v4u xt[4]; v4u PW_[4][2]; v2u pf, pb2, pc2; v4u xr0,xr1; v2u xr2;
  float rstd1, mean1; v4u MB1; f32x4 cv[3]; float cs[3]; v4u cf[3];
  auto xrd=[&](auto KC,auto SS) AI { constexpr int kc=KC, s=SS; ldsrdA<(kc%3)*4096>(c.X[kc*4+s],xr[s]); xt[s]=*(const LAS v4u*)(xr[s]+(kc%3)*4096); };
  auto csstep=[&](auto NT_,auto K) AI { constexpr int NT=NT_, k=K;
    if constexpr(k==0){ for(int t=0;t<NT;t++) cv[t]=*(const LAS f32x4*)(csr+t*512); }
    if constexpr(k==2){ for(int t=0;t<NT;t++){ cs[t]=cv[t][0]+cv[t][1]; PIN(cs[t]); cv[t][2]+=cv[t][3]; PIN(cv[t][2]); } }
    if constexpr(k==3){ for(int t=0;t<NT;t++){ cs[t]+=cv[t][2]; PIN(cs[t]); cf[t].x=pk2(cs[t],cs[t]); PIN(cf[t].x); } }
    if constexpr(k==4){ for(int t=0;t<NT;t++){ cf[t].y=cf[t].x<<16; PIN(cf[t].y); cs[t]-=__uint_as_float(cf[t].y); PIN(cs[t]); } }
    if constexpr(k==5){ for(int t=0;t<NT;t++){ cf[t].y=pk2(cs[t],0.f); pinV(cf[t]); } } };
  for(int t=0;t<3;t++){ cf[t]=v4u{0,0,0,0}; pinV(cf[t]); }
  vmwait<10>(); barrier();
  UNROLL(4,I,xrd(IC<0>{},IC<I>{}));
  pipe<150,4>(
    [&](auto i) AI { constexpr int I=i, kc=I/15, r=I%15, t=r%3, o0=(kc%4)*12288; unsigned a=(r<3? aw[t]+o0 : ab[t]+o0+((r-3)/3)*3072); return trfrag(a,a+768); },
    [&](auto i,v4u A) AI { constexpr int I=i, kc=I/15, r=I%15, t=r%3;
      if constexpr(r<3){ if constexpr(kc==0) mfma32A_0(acs[t],ONE,A); else mfma32A(acs[t],ONE,A); }
      else { if constexpr(kc==0 && r<6) mfma32_0(acc[t],A,c.X[0]); else mfma32(acc[t],A,c.X[kc*4+(r-3)/3]); } },
    [&](auto i) AI { constexpr int I=i, kc=I/15, r=I%15;
      constexpr int slot = r==0?0 : r==1?1 : r==3?2 : r==5?3 : r==6?4 : r==8?5 : r==10?6 : -1;
      if constexpr(kc<8 && slot>=0) ldp(kc+2,IC<(slot<0?0:slot)>{});
      if constexpr(I>=RT0 && I<RT0+8) rtp(IC<(I-RT0)&7>{});
      if constexpr(I==RT0+8) touch(dummy,ro,wov,((c.b>>3)*200+w*50)*128+(cc>>2)*640);
      if constexpr(I>=WQ0 && (I-WQ0)%WQS==0 && (I-WQ0)/WQS<WQN) wqp(IC<((I-WQ0)/WQS)%15>{});
      if constexpr(r==2){ for(int s=0;s<4;s++){ v4u v=xt[s]; sm[0]=dot2(v.x,C11,sm[0]); sq[0]=dot2(v.x,v.x,sq[0]); sm[1]=dot2(v.y,C11,sm[1]); sq[1]=dot2(v.y,v.y,sq[1]);
          sm[0]=dot2(v.z,C11,sm[0]); sq[0]=dot2(v.z,v.z,sq[0]); sm[1]=dot2(v.w,C11,sm[1]); sq[1]=dot2(v.w,v.w,sq[1]); } PIN(sm[0]); PIN(sm[1]); PIN(sq[0]); PIN(sq[1]); }
      if constexpr(r==11 && kc<9){
        if constexpr(kc==0) vmwait<8>(); else if constexpr(kc==8) vmwait<9>(); else vmwait<7>();
        barrier();
        if constexpr(kc==1){ asm volatile("" : "+v"(ep),"+v"(dummy));
          { u64 t=(((u64)ep.y<<32)|ep.x)+0x9E3779B97F4A7C15ull; c.tag=v2u{(unsigned)t,(unsigned)(t>>32)}; asm volatile("" : "+v"(c.tag)); }
          if(w==1) st8(c.tag,c.rs,c.flagv,WS_FLAG+FK_A*256*128); } }
      if constexpr(r>=11 && kc<9) xrd(IC<(kc+1)%10>{},IC<(r+1)%4>{});
      if constexpr(kc>=4 && kc<8 && (r==4||r==7||r==9)){ constexpr int a=kc-4;
        unsigned ho=((a<2? (pj&1):(jq&1))*2+(a&1))*800;
        if constexpr(r==4){ v2u v=trd(pwa+ho), u=trd(pwa+ho+320); PW_[a][0]=v4u{v.x,v.y,u.x,u.y}; }
        if constexpr(r==7){ v2u v=trd(pwb+ho+640); PW_[a][1]=v4u{v.x,v.y,0u,0u}; }
        if constexpr(r==9){ PW_[a][1].x= hh? 0u:PW_[a][1].x; PW_[a][1].y= hh? 0u:PW_[a][1].y; pinV(PW_[a][0]); pinV(PW_[a][1]); } }
      if constexpr(kc==9){
        if constexpr(r==3){ asm volatile("" : "+a"(acs[0]),"+a"(acs[1]),"+a"(acs[2])); for(int t=0;t<3;t++) *(LAS float*)(csw+t*512)=acs[t][0]; }
        if constexpr(r==4){ sm[0]+=sm[1]; sq[0]+=sq[1]; PIN(sm[0]); PIN(sq[0]); }
        if constexpr(r==5){ sm[0]=xh_add(sm[0]); sq[0]=xh_add(sq[0]); PIN(sm[0]); PIN(sq[0]); }
        if constexpr(r==6){ mean1=sm[0]*(-1.f/640.f); PIN(mean1); sq[0]=fmaxf(sq[0]*(1.f/640.f)-mean1*mean1,0.f)+1e-5f; PIN(sq[0]); }
        if constexpr(r==7){ lgk0(); barrier(); }
        if constexpr(r==8){ rstd1=__builtin_amdgcn_rsqf(sq[0]); PIN(rstd1); MB1.y=pk2(mean1,0.f); PIN(MB1.y); MB1.x=pk2(mean1,mean1-blo(MB1.y)); PIN(MB1.x); }
        if constexpr(r==13){ MB1= hh? v4u{0,0,0,0} : v4u{MB1.x,MB1.y,0,0}; pinV(MB1); }
        if constexpr(r>=7 && r<13) csstep(IC<3>{},IC<(r+5)%6>{});
      }
    });
  for(int t=0;t<3;t++) mfma32vv(acc[t],cf[t],MB1);
  { const float vs=hh? rstd1:1.f;
    f32x16 pz[2]; v4u k0[2],k1[2];
    UNROLL(7-WQN,I,wqp(IC<WQN+I>{}));
    asm volatile("s_nop 7" : "+v"(acc[0]),"+v"(acc[1]));
    f32x2 m_[3][4];
    for(int a=0;a<2;a++) k0[a]=pack8r(acc[a],0);
    SB0();
    for(int a=0;a<2;a++) for(int e=0;e<4;e++) m_[a][e]=f32x2{acc[a][8+2*e],acc[a][9+2*e]}*(e<2? vs:rstd1);
    SB0();
    for(int a=0;a<2;a++){ k1[a]=v4u{pk2(m_[a][0][0],m_[a][0][1]),pk2(m_[a][1][0],m_[a][1][1]),pk2(m_[a][2][0],m_[a][2][1]),pk2(m_[a][3][0],m_[a][3][1])}; }
    asm volatile("" : "+v"(acc[2]));
    for(int e=0;e<4;e++) m_[2][e]=f32x2{acc[2][2*e],acc[2][2*e+1]}*rstd1;
    for(int a=0;a<2;a++){ pinV(k0[a]); pinV(k1[a]); }
    SB0();
    for(int a=0;a<2;a++){ mfma32vv_0(pz[a],PW_[a][0],k0[a]); mfma32vv(pz[a],PW_[a][1],k1[a]); }
    SB0();
    UNROLL(3,I,wqp(IC<7+I>{}));
    for(int t=0;t<2;t++){ *(LAS v2u*)(vwa[t]) = v2u{sel(hh,k1[t].x,0x00003f80u),sel(hh,k1[t].y,0u)}; *(LAS v2u*)(vwb[t]) = v2u{k1[t].z,k1[t].w}; }
    *(LAS v2u*)(vwb[1]+48) = v2u{pk2(m_[2][0][0],m_[2][0][1]),pk2(m_[2][1][0],m_[2][1][1])}; *(LAS v2u*)(vwb[1]+64) = v2u{pk2(m_[2][2][0],m_[2][2][1]),pk2(m_[2][3][0],m_[2][3][1])};
    SB0(); asm volatile("" : "+v"(pz[0]),"+v"(pz[1]));
    unsigned ph[2][10]; hedgehog2(pz[0],pz[1],hh,rstd1*LOG2E,ph,[&](int k) AI {
      if(k==0){ UNROLL(3,I,wqp(IC<10+I>{})); }
      if(k==1){ UNROLL(2,I,wqp(IC<13+I>{})); }
      if(k==2){ vmwait<11>(); UNROLL(5,J,ldswrA<J*1024>(rw3,RG[J])); }
      if(k==3){ UNROLL(3,J,ldswrA<J*1024>(rw4,RG[5+J]));
        asm volatile("buffer_load_dwordx4 %0, %1, %2, %3 offen" : "=v"(xr0) : "v"(xo_), "s"(rx), "s"(t16*20480+cc*160) : "memory");
        asm volatile("buffer_load_dwordx4 %0, %1, %2, %3 offen offset:64" : "=v"(xr1) : "v"(xo_), "s"(rx), "s"(t16*20480+cc*160) : "memory");
        asm volatile("buffer_load_dwordx2 %0, %1, %2, %3 offen offset:128" : "=v"(xr2) : "v"(xo2), "s"(rx), "s"(t16*20480+cc*160) : "memory"); } }, [&](auto) AI {});
    for(int a=0;a<2;a++) for(int i=0;i<5;i++) *(LAS v2u*)(prow+a*80+i*8)=v2u{ph[a][2*i],ph[a][2*i+1]};
  }
  vmwait<3>(); lgk0(); barrier();
  PIN(dummy);
  { v4u fa[8],fb[8];
    #pragma unroll
    for(int s=0;s<3;s++){ fa[s]=trfrag(pa+s*2688,pa+s*2688+672); fb[s]=trfrag(va+s*1664,va+s*1664+416); }
    SB0();
    f32x16 kv;
    #pragma unroll
    for(int s=0;s<8;s++){
      if(s==0) mfma32vv_0(kv,fa[0],fb[0]); else mfma32vv(kv,fa[s],fb[s]);
      SB0();
      if(s+3<8){ fa[s+3]=trfrag(pa+(s+3)*2688,pa+(s+3)*2688+672); fb[s+3]=trfrag(va+(s+3)*1664,va+(s+3)*1664+416); }
      SB0(); }
    mdone(kv);
    const unsigned so=WS_KVP+(pj*16+pi)*KVP_B+w*896;
    st8(v2u{pk2(kv[0],kv[1]),pk2(kv[2],kv[3])},c.rs,kvs,so);
    st8(v2u{pk2(kv[4],kv[5]),pk2(kv[6],kv[7])},c.rs,kvs,so+352);
    st8(v2u{pk2(kv[8],kv[9]),pk2(kv[10],kv[11])},c.rs,kvs2,so+704);
  }
  unsigned phq[2][10]; v4u pb[2][3]; v2u wf1; unsigned kra[2], wb[2][3], kia[2];
  const unsigned wfo1=(4*min(lane,53)+(cc>>1))*128;
  { f32x16 aq[2], rcs; v4u rf[8]; v4u kp[16]; v4u IDN;
    constexpr int B2=80, SP=3, W1=50, K0=68, KS=2;
    static_assert(1+15*SP<W1 && W1+12<B2 && B2+7<100 && K0>W1+13 && K0+15*KS<100 && B2<=K0+16*KS && 1+7*SP>=14, "Q pass schedule");
    pipe<100,4>(
      [&](auto i) AI { constexpr int I=i; unsigned a=(I<20? qw[I&1]:qb[I&1])+(I<20? I/2:(I-20)/2)*1280; return trfrag(a,a+80); },
      [&](auto i,v4u A) AI { constexpr int I=i;
        if constexpr(I<20){ if constexpr(I<2) mfma32A_0(acs[I&1],ONE,A); else mfma32A(acs[I&1],ONE,A); }
        else { if constexpr(I<22) mfma32_0(aq[I&1],A,c.X[0]); else mfma32(aq[I&1],A,c.X[(I-20)/2]); } },
      [&](auto i) AI { constexpr int I=i;
        if constexpr(I==0) ld8(pf,c.rs,pfo,WS_FLAG+(FK_A*256+jq)*128);
        if constexpr(I<5) rf[I&7]=trfrag(r3+I*2048,r3+I*2048+512);
        if constexpr(I==5) rf[5]=trfrag(r4a,r4a+640);
        if constexpr(I==6) rf[6]=trfrag(r4a+8,r4a+648);
        if constexpr(I==7) rf[7]=trfrag(r4b,r4b+640);
        if constexpr(I>=5 && I<10){ if constexpr(I==5) mfma32vv_0(rcs,ONE,rf[0]); else mfma32vv(rcs,ONE,rf[(I-5)&7]); }
        if constexpr(I>=1 && (I-1)%SP==0 && (I-1)/SP<16){ constexpr int n=(I-1)/SP;
          if constexpr(n==0) st16(PZ,c.rs,ov1,osb);
          if constexpr(n==1) st16(PZ,c.rs,ov1,osb+768);
          if constexpr(n==2) st16(PZ,c.rs,ov2,osb);
          if constexpr(n==3) st16(PZ,c.rs,hv1,hsb);
          if constexpr(n==4) st16(PZ,c.rs,hv1,hsb+2048);
          if constexpr(n==5) st16(PZ,c.rs,hv2,hsb+4096);
          if constexpr(n==6) st8(v2u{PZ.x,PZ.y},c.rs,sv,ssb);
          if constexpr(n==7){ asm volatile("" : "+v"(rcs)); float ct=rcs[0]; unsigned hi=pk2(ct,0.f); st4(pk2(ct,ct-blo(hi)),c.rs,csv,dcs); }
          if constexpr(n>=8 && n<13) st16(rf[(n-8)&7],c.rs,c.voff,d3+(n-8)*d3s);
          if constexpr(n==13) st16(rf[5],c.rs,c.voff,d4+(w&1)*2048);
          if constexpr(n==14) st16(rf[6],c.rs,c.voff,d4+(w&1)*2048+1024);
          if constexpr(n==15) st16(rf[7],c.rs,c.voff,(w&1)? d4+4096:WS_END); }
        if constexpr(I==27){ asm volatile("" : "+a"(acs[0]),"+a"(acs[1])); for(int a=0;a<2;a++) *(LAS float*)(csw+a*512)=acs[a][0]; }
        if constexpr(I==B2){ vmwait<5+(B2>K0? (B2-K0+KS-1)/KS:0)>(); barrier(); if(w==2) st8(c.tag,c.rs,c.flagv,WS_FLAG+FK_B*256*128); }
        if constexpr(I>=W1 && (I-W1)%3==0 && (I-W1)/3<5) wop(IC<((I-W1)/3)%25>{});
        if constexpr(I==K0-1){ vmwait<21>(); asm volatile("" : "+v"(pf),"+v"(xr0),"+v"(xr1),"+v"(xr2));
          _Pragma("nounroll") for(;;){
            if(!__any(((pf.x^c.tag.x)|(pf.y^c.tag.y))!=0) || --c.budget<0) break;
            ld8(pf,c.rs,pfo,WS_FLAG+(FK_A*256+jq)*128); vmwait<0>(); asm volatile("" : "+v"(pf)); } }
        if constexpr(I>=K0 && I<K0+16*KS && (I-K0)%KS==0) ldV<0>(kp[((I-K0)/KS)&15],c.rs,kvo,WS_KVP+(jq*16+(((I-K0)/KS)&15))*KVP_B);
        if constexpr(I==W1+2){ const unsigned e=tok-8*hh, one=sel(e&1,0x3f800000u,0x3f80u);
          IDN=v4u{sel(e>>1==0,one,0),sel(e>>1==1,one,0),sel(e>>1==2,one,0),sel(e>>1==3,one,0)}; pinV(IDN); }
        if constexpr(I>=B2 && I<B2+6) csstep(IC<2>{},IC<(I-B2+600)%6>{});
        if constexpr(I==W1+1){ for(int z=0;z<2;z++){ int ch=tok+32*z, qu=(ch*187)>>11, dp=ch-qu*11; kia[z]=sel(ch<55,M_KVI+(((w>>1)*22+2*dp+hh)*2+(w&1))*48+qu*8,M_DUM); PIN(kia[z]); } }
        if constexpr(I==W1+4 || I==W1+7){ constexpr int a=(I-W1-4)/3; const int h2=(tok>>2)&1, q=(tok&3)+4*(tok>>3);
          unsigned d=sel(q==12,20,21); d=sel(q<12 && !h2,a==0? 8+q:q-8,d); d=sel(q<8,4*a+8*h2+q,d); kra[a&1]=M_KVI+a*2112+d*96+hh*48; PIN(kra[a&1]); }
        if constexpr(I==W1+10){ const unsigned rb=M_WO+160*(16*hh+4*blk+q4);
          wb[0][0]=rb+16*p4+8*(p4>>1); wb[0][1]=wb[0][0]^8; wb[0][2]=rb+128+8*p4; for(int t=0;t<3;t++) PIN(wb[0][t]); }
        if constexpr(I==W1+11){ for(int t=0;t<3;t++){ wb[1][t]=wb[0][t]+61440; PIN(wb[1][t]); } }
      });
    for(int a=0;a<2;a++) mfma32vv(aq[a],cf[a],MB1);
    UNROLL(3,J,wop(IC<5+J>{}));
    f32x16 pq[2], ks; v4u q0[2],q1[2]; unsigned km=0;
    asm volatile("s_nop 3" : "+v"(aq[0]),"+v"(aq[1]));
    for(int a=0;a<2;a++){ q0[a]=pack8r(aq[a],0); q1[a]=v4u{pk2(aq[a][8],aq[a][9]),pk2(aq[a][10],aq[a][11]),0,0}; }
    for(int a=0;a<2;a++){ pinV(q0[a]); pinV(q1[a]); }
    nop2();
    for(int a=0;a<2;a++){ mfma32vv_0(pq[a],PW_[2+a][0],q0[a]); mfma32vv(pq[a],PW_[2+a][1],q1[a]); }
    auto red=[&](auto i) AI { constexpr int I=i; if constexpr(I==0) mfma32vv_0(ks,IDN,kp[0]); else mfma32vv(ks,IDN,kp[I]);
      umax3(km,kp[I].x,kp[I].z); };
    vmwait<3+10>(); red(IC<0>{}); red(IC<1>{}); red(IC<2>{});
    UNROLL(2,J,wop(IC<8+J>{}));
    asm volatile("s_nop 1" : "+v"(pq[0]),"+v"(pq[1]));
    hedgehog2(pq[0],pq[1],hh,rstd1*LOG2E,phq,[&](int k) AI {
      if(k==0){ wop(IC<10>{}); wop(IC<11>{}); }
      if(k==1){ wop(IC<12>{}); wop(IC<13>{}); wop(IC<14>{}); }
      if(k==2){ wop(IC<15>{}); wop(IC<16>{}); wop(IC<17>{}); }
      if(k==3){ wop(IC<18>{}); wop(IC<19>{}); } },
      [&](auto m) AI { constexpr int M=m; if constexpr(M==3) vmwait<7>(); red(IC<3+M>{}); });
    for(int a=0;a<2;a++){ pb[a][0]=v4u{phq[a][0],phq[a][1],phq[a][2],phq[a][3]}; pb[a][1]=v4u{phq[a][4],phq[a][5],phq[a][6],phq[a][7]}; pb[a][2]=v4u{phq[a][8],phq[a][9],0,0}; for(int s=0;s<3;s++) pinV(pb[a][s]); }
    _Pragma("nounroll") for(;;){
      if(!__any(km==POISON) || --c.budget<0) break;
      UNROLL(16,I,ldV<0>(kp[I],c.rs,kvo,WS_KVP+(jq*16+I)*KVP_B)); vmwait<0>();
      km=0; PIN(km); UNROLL(16,I,red(IC<I>{})); mdone(ks);
    }
    asm volatile("" : "+v"(ks));
    *(LAS v2u*)kia[0]=v2u{pk2(ks[0],ks[1]),pk2(ks[2],ks[3])}; *(LAS v2u*)kia[1]=v2u{pk2(ks[4],ks[5]),pk2(ks[6],ks[7])};
    ld8(pb2,c.rs,pbo,WS_FLAG+FK_B*256*128); ld8(pc2,c.rs,pco,WS_FLAG+FK_B*256*128);
    vmwait<7>(); lgk0(); barrier();
  }
  { v4u ka[2][3];
    for(int a=0;a<2;a++) for(int s=0;s<3;s++) ka[a][s]=*(const LAS v4u*)(kra[a]+16*s);
    SB0();
    f32x16 o[2];
    for(int a=0;a<2;a++){ mfma32vv_0(o[a],ka[a][0],pb[a][0]); mfma32vv(o[a],ka[a][1],pb[a][1]); mfma32vv(o[a],ka[a][2],pb[a][2]); }
    mdone(o[1]); asm volatile("" : "+v"(o[0]));
    float r[2]; for(int a=0;a<2;a++) r[a]=__builtin_amdgcn_rcpf(o[a][12]+1e-6f);
    SB0();
    f32x2 y[2][6]; for(int a=0;a<2;a++) for(int e=0;e<6;e++) y[a][e]=f32x2{o[a][2*e],o[a][2*e+1]}*r[a];
    SB0();
    unsigned u_[2][6]; for(int a=0;a<2;a++) for(int e=0;e<6;e++) u_[a][e]=pk2(y[a][e][0],y[a][e][1]);
    st16(v4u{u_[0][0],u_[0][1],u_[0][2],u_[0][3]},c.rs,ov1,osb); st16(v4u{u_[1][0],u_[1][1],u_[1][2],u_[1][3]},c.rs,ov1,osb+768); st16(v4u{u_[0][4],u_[0][5],u_[1][4],u_[1][5]},c.rs,ov2,osb);
  }
  v4u hr0,hr1; v2u hr2;
  { unsigned pr, prv=tk*1280+512+240;
    v4u WB[4][5]; v4u O[20]; const unsigned oo=WS_O+t16*20480;
    auto oload=[&](auto i) AI { constexpr int s=i; int sp=s+orot; sp= sp>=20? sp-20:sp; ldV<0>(O[s],c.rs,c.voff,oo+sp*1024); };
    UNROLL(5,I,wop(IC<20+I>{}));
    vmwait<8>(); asm volatile("" : "+v"(pb2),"+v"(pc2));
    _Pragma("nounroll") for(;;){
      if(!__any(((pb2.x^c.tag.x)|(pb2.y^c.tag.y)|(pc2.x^c.tag.x)|(pc2.y^c.tag.y))!=0) || --c.budget<0) break;
      ld8(pb2,c.rs,pbo,WS_FLAG+FK_B*256*128); ld8(pc2,c.rs,pco,WS_FLAG+FK_B*256*128); vmwait<0>(); asm volatile("" : "+v"(pb2),"+v"(pc2)); }
    v4u WA[12][5];
    auto pre=[&](auto i) AI { constexpr int s=i;
      if constexpr(s==12) barrier();
      #pragma unroll
      for(int u=0;u<5;u++){
        if constexpr(s<12){ unsigned a=wb[0][u<4? u&1:2]+s*5120+(u<4? 64*(u>>1):0); WA[s][u]=trfrag(a,a+1280); }
        else if constexpr(s<16){ unsigned a=wb[1][u<4? u&1:2]+(s-12)*5120+(u<4? 64*(u>>1):0); WB[s-12][u]=trfrag(a,a+1280); } }
      SB0();
      if constexpr(s<RP1) issue_piece<s>(c);
      #pragma unroll
      for(int u=0;u<5;u++){ if constexpr(s>=1 && s<13) pinA(WA[s-1][u]); else if constexpr(s>=13) pinV(WB[s-13][u]); }
      SB0(); };
    UNROLL(17,I,pre(IC<I>{}));
    auto again=[&]() AI {
      _Pragma("nounroll") for(;;){
        asm volatile("buffer_load_dword %0, %1, %2, %3 offen sc1\n\ts_waitcnt vmcnt(0)" : "=v"(pr) : "v"(prv), "s"(c.rs), "s"(oo) : "memory");
        if(!__any(pr==POISON) || --c.budget<0) break;
        __builtin_amdgcn_s_sleep(2); }
      UNROLL(12,J,oload(IC<J>{})); };
    again();
    barrier();
    f32x4 a2[5];
    v4u WC[4][5];
    unsigned omax;
    auto p2step=[&](auto i) AI { constexpr int I=i, s=I/5, u=I%5;
      if constexpr(u==0 && s%2==0) vmwait<(s<=9+RP2? 10:19+RP2-s)>();
      if constexpr(s==0) mfma16a_0(a2[u],WA[0][u],O[0]);
      else if constexpr(s<12) mfma16a(a2[u],WA[s<12?s:0][u],O[s]);
      else if constexpr(s<16) mfma16(a2[u],WB[(s-12)&3][u],O[s]);
      else mfma16(a2[u],WC[(s-16)&3][u],O[s]);
      SB0();
      if constexpr(u==1){ if constexpr(s<8) oload(IC<(12+s)%20>{}); else if constexpr(s==8) ld8(wf1,c.rs,wfo1,WS_FLAG+FK_B*256*128); else if constexpr(s<9+RP2) issue_piece<(s+RP1-9)>(c); }
      if constexpr(u==3 && s%2==0) asm volatile("v_max3_u32 %0, %0, %1, %2" : "+v"(omax) : "v"(O[s].x), "v"(O[s+1].x));
      if constexpr(I>=20 && I<60 && I%2==0){ constexpr int n=(I-20)/2, s2=n/5, u2=n%5; unsigned a=wb[1][u2<4? u2&1:2]+(4+s2)*5120+(u2<4? 64*(u2>>1):0); WC[s2][u2]=trfrag(a,a+1280); }
      SB0(); };
    for(;;){
      omax=0;
      UNROLL(100,I,p2step(IC<I>{}));
      if(!__any(omax==POISON) || --c.budget<0) break;
      vmwait<0>(); asm volatile("s_nop 7" ::: "memory");
      again(); vmwait<0>(); UNROLL(12,J,pinV(O[J]));
    }
    f32x2 xf[10]; { unsigned xw[10]={xr0.x,xr0.y,xr0.z,xr0.w,xr1.x,xr1.y,xr1.z,xr1.w,xr2.x,xr2.y}; for(int i=0;i<10;i++) xf[i]=f32x2{blo(xw[i]),bhi(xw[i])}; }
    SB0();
    asm volatile("" : "+v"(a2[0]),"+v"(a2[1]),"+v"(a2[2]),"+v"(a2[3]),"+v"(a2[4]));
    const bool sw=g4>=2;
    float hv_[20];
    for(int r=0;r<4;r++){ hv_[r]= sw? a2[1][r]:a2[0][r]; hv_[4+r]= sw? a2[0][r]:a2[1][r]; hv_[8+r]= sw? a2[3][r]:a2[2][r]; hv_[12+r]= sw? a2[2][r]:a2[3][r]; hv_[16+r]=a2[4][r]; }
    SB0();
    for(int i=0;i<10;i++) xf[i]+=f32x2{hv_[2*i],hv_[2*i+1]};
    SB0();
    unsigned hw[10]; for(int i=0;i<10;i++) hw[i]=pk2(xf[i][0],xf[i][1]);
    hr0=v4u{hw[0],hw[1],hw[2],hw[3]}; hr1=v4u{hw[4],hw[5],hw[6],hw[7]}; hr2=v2u{hw[8],hw[9]};
    st16(hr0,c.rs,hv1,hsb); st16(hr1,c.rs,hv1,hsb+2048);
    { auto sx=__builtin_amdgcn_permlane16_swap(hr2.x,hr2.x,false,false); auto sy=__builtin_amdgcn_permlane16_swap(hr2.y,hr2.y,false,false);
      st16(v4u{sx[0],sy[0],sx[1],sy[1]},c.rs,hv2,hsb+4096); }
    float s1=0.f,s1b=0.f; for(int i=0;i<5;i++){ s1=dot2(hw[2*i],C11,s1); s1b=dot2(hw[2*i+1],C11,s1b); }
    for(int i=0;i<10;i++) xf[i]=f32x2{blo(hw[i]),bhi(hw[i])};
    s1=xh_add(x16_add(s1+s1b))*(1.f/80.f);
    SB0();
    for(int i=0;i<10;i++) xf[i]-=s1;
    SB0();
    f32x2 s2={0.f,0.f}, s3={0.f,0.f}; for(int i=0;i<5;i++){ s2=__builtin_elementwise_fma(xf[2*i],xf[2*i],s2); s3=__builtin_elementwise_fma(xf[2*i+1],xf[2*i+1],s3); }
    s2+=s3;
    float s2t=xh_add(x16_add(s2[0]+s2[1]));
    st8(v2u{__float_as_uint(s1),__float_as_uint(s2t)},c.rs,sv,ssb);
  }
  vmwait<4+RP2>(); asm volatile("" : "+v"(wf1));
  UNROLL(NPRE-RP1-RP2,I,issue_piece<RP1+RP2+I>(c));
  _Pragma("nounroll") for(;;){
    if(!__any(((wf1.x^c.tag.x)|(wf1.y^c.tag.y))!=0) || --c.budget<0) break;
    ld8(wf1,c.rs,wfo1,WS_FLAG+FK_B*256*128); vmwait<0>(); asm volatile("" : "+v"(wf1));
  }
  for(int i=0;i<3;i++){ c.lp[i]=lane*16 + i*65536; c.lq[i]=lane*16 + i*61440 + kh*5120; asm volatile("" : "+v"(c.lp[i]), "+v"(c.lq[i])); }
  c.actst = WS_ACT + ((pi*4+w)*2+blk)*55296 + hh*256 + tk*16; PIN(c.actst);
  c.actld = WS_ACT + (pi*8+rh*4+2*th)*55296 + kh*1024;
  c.av[0]=lane*16+kh*55296; c.av[1]=lane*16+(1-kh)*55296; PIN(c.av[0]); PIN(c.av[1]);
  v2u stt[4];
  c.xo = WS_H + (pi*4+w)*40960; c.rot=cc; c.pollv=(pi*16+tk)*128; PIN(c.pollv);
  unsigned stv=(4*hh)*1024+(32*w+tok)*8; PIN(stv);
  auto window=[&]() AI {
    _Pragma("nounroll") for(;;){
      for(int q=0;q<4;q++) ld8(stt[q],c.rs,stv,WS_ST+pi*8192+q*1024);
      vmwait<0>();
      asm volatile("" : "+v"(stt[0]),"+v"(stt[1]),"+v"(stt[2]),"+v"(stt[3]));
      if(!__any(max(max(stt[0].x,stt[1].x),max(stt[2].x,stt[3].x))==POISON) || --c.budget<0) break;
      __builtin_amdgcn_s_sleep(2);
    }
    UNROLL(XPRE,I,xload<I>(c)); };
  Epi ew;
  for(int sp=0;;++sp){
    window();
    if(sp==0) barrier();
    for(int r=0;r<RA;r++) c.A[r]=ldsA(c,r);
    { float m0=__uint_as_float(stt[0].x),m1=__uint_as_float(stt[1].x),m2=__uint_as_float(stt[2].x),m3=__uint_as_float(stt[3].x);
      float mean=xh_add((m0+m1)+(m2+m3))*0.125f;
      float q=(__uint_as_float(stt[0].y)+__uint_as_float(stt[1].y))+(__uint_as_float(stt[2].y)+__uint_as_float(stt[3].y));
      q += 80.f*(((m0-mean)*(m0-mean)+(m1-mean)*(m1-mean))+((m2-mean)*(m2-mean)+(m3-mean)*(m3-mean)));
      q = xh_add(q);
      c.var=q*(1.f/640.f)+1e-5f; float rs=__builtin_amdgcn_rsqf(c.var); c.c1r=GC1*rs; c.c2r=GC2*rs*rs*rs;
      unsigned mb=pk2(-mean,-mean); c.MB=v4u{mb,mb,mb,mb}; pinV(c.MB); PIN(c.var); PIN(c.c1r); PIN(c.c2r);
    }
    c.xmax=0;
    vmwait<XPRE-1>();
    SB0();
    UNROLL(G1,I,gapx<I>(c,ew));
    if(__builtin_expect(!__any(c.xmax==POISON) || --c.budget<0,1)) break;
    vmwait<0>(); lgk0(); asm volatile("s_nop 11" ::: "memory");
  }
  UNROLL(GN-G1,I,gapx<G1+I>(c,ew));
  { f32x2 hf[10]; { unsigned hw[10]={hr0.x,hr0.y,hr0.z,hr0.w,hr1.x,hr1.y,hr1.z,hr1.w,hr2.x,hr2.y}; for(int i=0;i<10;i++) hf[i]=f32x2{blo(hw[i]),bhi(hw[i])}; }
    SB0();
    asm volatile("" : "+v"(c.acc4[1][0]),"+v"(c.acc4[1][1]),"+v"(c.acc4[1][2]),"+v"(c.acc4[1][3]),"+v"(c.acc4[1][4]));
    for(int u=0;u<5;u++) *(LAS f32x4*)(c.lp[0]+w*5120+u*1024) = c.acc4[1][u];
    asm volatile("" : "+v"(c.acc4[0][0]),"+v"(c.acc4[0][1]),"+v"(c.acc4[0][2]),"+v"(c.acc4[0][3]),"+v"(c.acc4[0][4]));
    for(int i=0;i<10;i++) hf[i]+=f32x2{c.acc4[0][i/2][2*(i&1)],c.acc4[0][i/2][2*(i&1)+1]};
    const v4u rout=mkrs(ar.out,S*D*2);
    lgk0(); barrier();
    f32x4 r[5]; for(int u=0;u<5;u++) r[u]=*(const LAS f32x4*)(c.lp[0]+(w^1)*5120+u*1024);
    unsigned ow[10];
    for(int i=0;i<10;i++){ f32x2 y=hf[i]+f32x2{r[i/2][2*(i&1)],r[i/2][2*(i&1)+1]}; ow[i]=pk2(y[0],y[1]); }
    const unsigned so=t16*20480+cc*160, vo=tk*1280+g4*16;
    asm volatile("buffer_store_dwordx4 %0, %1, %2, %3 offen" :: "v"(v4u{ow[0],ow[1],ow[2],ow[3]}), "v"(vo), "s"(rout), "s"(so) : "memory");
    asm volatile("buffer_store_dwordx4 %0, %1, %2, %3 offen offset:64" :: "v"(v4u{ow[4],ow[5],ow[6],ow[7]}), "v"(vo), "s"(rout), "s"(so) : "memory");
    asm volatile("buffer_store_dwordx2 %0, %1, %2, %3 offen offset:128\n\ts_nop 1" :: "v"(v2u{ow[8],ow[9]}), "v"(vo-g4*8), "s"(rout), "s"(so) : "memory");
  }
  if(lid==0 && w==0) st8(c.tag,c.rs,c.flagv,WS_EPOCH);
  asm volatile(".Lhg_code_end:");
}

extern "C" void kernel_launch(void* const* d_in, const int* in_sizes, int n_in,
                              void* d_out, int out_size, void* d_ws, size_t ws_size,
                              hipStream_t stream) {
  Args a; a.x=(const u16*)d_in[0]; a.wqkv=(const u16*)d_in[1]; a.wphi=(const u16*)d_in[2]; a.wo=(const u16*)d_in[3];
  a.wg=(const u16*)d_in[5]; a.wu=(const u16*)d_in[6]; a.wd=(const u16*)d_in[7]; a.out=(u16*)d_out; a.ws=(char*)d_ws;
  (void)hipFuncSetAttribute((const void*)blk_k, hipFuncAttributeMaxDynamicSharedMemorySize, 163840);
  blk_k<<<256,256,163840,stream>>>(a);
}
```
